```python
import torch

def _softmax(x, axis=-1):
    return torch.softmax(x, dim=axis)
def _gelu(x):
    return torch.nn.functional.gelu(x, approximate="tanh")
def _silu(x):
    return torch.nn.functional.silu(x)
def _sigmoid(x):
    return torch.sigmoid(x)
def _layernorm(x, axis=-1, eps=1e-5):
    m = x.mean(dim=axis, keepdim=True)
    v = x.var(dim=axis, keepdim=True, unbiased=False)
    return (x - m) / torch.sqrt(v + eps)
def _scatter_add(tgt, idx, src, axis=0):
    return tgt.clone().index_add_(axis, idx, src)
def _segment_sum(src, seg, n):
    out = torch.zeros((n,) + src.shape[1:], dtype=src.dtype)
    return out.index_add_(0, seg, src)
def _pad(x, pw):
    flat = tuple(v for pair in reversed(pw) for v in pair)
    return torch.nn.functional.pad(x, flat)

kernel_name = 'standalone__gqa_block_cached__sampling'

def setup_inputs():

    _w = (lambda a: (lambda t: t.to(torch.bfloat16) if t.is_floating_point() else t)(torch.as_tensor(a)))
    B, Sq, D = 32, 32, 1024
    Hq, Hkv, Sk, Dh = 32, 4, 2048, 128
    assert Hq % Hkv == 0, (Hq, Hkv)
    return {
        "x": _w((lambda *s: torch.randn(*s, dtype=torch.bfloat16) * 0.8)(B, Sq, D)),
        "k_cache": _w((lambda *s: torch.randn(*s, dtype=torch.bfloat16) * 0.8)(B, Hkv, Sk, Dh)),
        "v_cache": _w((lambda *s: torch.randn(*s, dtype=torch.bfloat16) * 0.05)(B, Hkv, Sk, Dh)),
        "wq": _w((lambda *s: torch.randn(*s, dtype=torch.bfloat16) * 0.8)(D, Hq * Dh) * (D ** -0.5)),
        "wo": _w((lambda *s: torch.randn(*s, dtype=torch.bfloat16) * 0.05)(Hq * Dh, D) * ((Hq * Dh) ** -0.5)),
    }

def reference(x, k_cache, v_cache, wq, wo):
    B, Sq, D = x.shape
    Hq, Dh = 32, 128
    Hkv = k_cache.shape[1]
    rep = Hq // Hkv
    q = (lambda x, *d: x.permute(*d))((x @ wq).reshape(B, Sq, Hq, Dh), 0, 2, 1, 3)
    k = (lambda x, n, axis: torch.repeat_interleave(x, n, dim=axis))(k_cache, rep, axis=1)
    v = (lambda x, n, axis: torch.repeat_interleave(x, n, dim=axis))(v_cache, rep, axis=1)
    scores = torch.einsum("bhqd,bhkd->bhqk", q, k) * (Dh ** -0.5)
    p = _softmax(scores, axis=-1)
    o = torch.einsum("bhqk,bhkd->bhqd", p, v)
    return (lambda x, *d: x.permute(*d))(o, 0, 2, 1, 3).reshape(B, Sq, Hq * Dh) @ wo
```

```cpp
#include <hip/hip_runtime.h>
#include <hip/hip_bf16.h>
#include <stdint.h>

typedef __attribute__((ext_vector_type(8))) __bf16 bf16x8;
typedef __attribute__((ext_vector_type(4))) __bf16 bf16x4;
typedef __attribute__((ext_vector_type(2))) __bf16 bf16x2;
typedef __attribute__((ext_vector_type(16))) float f32x16;
typedef __attribute__((ext_vector_type(4))) float f32x4;
typedef __attribute__((ext_vector_type(2))) float f32x2;
typedef __attribute__((ext_vector_type(4))) unsigned u32x4;
typedef __attribute__((ext_vector_type(2))) unsigned u32x2;

#define DI __device__ __forceinline__
#define LDSP(T, a) ((__attribute__((address_space(3))) T*)(uintptr_t)(a))
#define SBAR() __builtin_amdgcn_sched_barrier(0)

constexpr int NB_ = 32, SQ_ = 32, DM_ = 1024, HQ_ = 32, HKV_ = 4, SK_ = 2048, DH_ = 128;
constexpr int MROWS = NB_ * SQ_;
constexpr int QCOLS = HQ_ * DH_;
constexpr float QSCALE = 0.08838834764831845f * 1.4426950408889634f;

DI unsigned cvtpk(float lo, float hi) {
  f32x2 v = {lo, hi}; bf16x2 b = __builtin_convertvector(v, bf16x2); return __builtin_bit_cast(unsigned, b);
}
DI float max3f(float a, float b, float c) { float r; asm("v_max3_f32 %0, %1, %2, %3" : "=v"(r) : "v"(a), "v"(b), "v"(c)); return r; }
DI float max2f(float a, float b) { float r; asm("v_max_f32_e32 %0, %1, %2" : "=v"(r) : "v"(a), "v"(b)); return r; }
DI f32x16 mfma32(bf16x8 a, bf16x8 b, f32x16 c) { return __builtin_amdgcn_mfma_f32_32x32x16_bf16(a, b, c, 0, 0, 0); }

template <int IMM> DI void dma16(unsigned m0v, unsigned voff, __amdgpu_buffer_rsrc_t srd, unsigned soff) {
  asm volatile("s_mov_b32 m0, %0\n\ts_nop 0\n\tbuffer_load_dwordx4 %1, %2, %3 offen offset:%4 lds"
               :: "s"(m0v), "v"(voff), "s"(srd), "s"(soff), "i"(IMM) : "memory");
}
DI bf16x8 lds_b128(unsigned a) { return *LDSP(const bf16x8, a); }
DI bf16x4 lds_tr(unsigned a) { return __builtin_amdgcn_ds_read_tr16_b64_v4bf16(LDSP(bf16x4, a)); }
DI bf16x8 cat8(bf16x4 a, bf16x4 b) { return __builtin_shufflevector(a, b, 0, 1, 2, 3, 4, 5, 6, 7); }

template <int I> struct IC { static constexpr int value = I; };
template <int B, int E, class F> DI void static_for(F&& f) {
  if constexpr (B < E) { f(IC<B>{}); static_for<B + 1, E>(static_cast<F&&>(f)); }
}

constexpr int G_NS = 4;
constexpr int G_STAGE = 32768;

template <int LDA, int LDB, int KLEN>
DI void gemm_tile(const __hip_bfloat16* __restrict__ Ag, unsigned Abytes, const __hip_bfloat16* __restrict__ Bg, unsigned Bbytes,
                  unsigned ldsbase, f32x16 (&acc)[2][2]) {
  constexpr int NT = KLEN / 64;
  const int tid = threadIdx.x, lane = tid & 63;
  const int wid = __builtin_amdgcn_readfirstlane(tid >> 6);
  const int wm = wid >> 1, wn = wid & 1;
  auto srdA = __builtin_amdgcn_make_buffer_rsrc((void*)Ag, 0, Abytes, 0x00020000);
  auto srdB = __builtin_amdgcn_make_buffer_rsrc((void*)Bg, 0, Bbytes, 0x00020000);
  const unsigned voffA0 = (unsigned)((lane >> 3) * LDA * 2 + ((((lane & 7) ^ ((lane >> 4) & 7))) << 4));
  const unsigned voffA1 = (unsigned)((lane >> 3) * LDA * 2 + ((((lane & 7) ^ ((4 + (lane >> 4)) & 7))) << 4));
  const unsigned voffB = (unsigned)((lane >> 4) * LDB * 2 + (((lane & 15) ^ (((lane >> 4) & 3) << 2)) << 4));
  auto issue = [&](int kt, unsigned stg  ) {
    const unsigned mA = ldsbase + stg + wid * 4096, mB = ldsbase + stg + 16384 + wid * 4096;
    const unsigned sA = (unsigned)(kt * 128 + wid * 4 * 16 * LDA), sB = (unsigned)((kt * 64 + wid * 16) * LDB * 2);
    dma16<0>(mA, voffA0, srdA, sA);
    dma16<0>(mA + 1024, voffA1, srdA, sA + 16 * LDA);
    dma16<0>(mA + 2048, voffA0, srdA, sA + 32 * LDA);
    dma16<0>(mA + 3072, voffA1, srdA, sA + 48 * LDA);
    dma16<0>(mB, voffB, srdB, sB);
    dma16<0>(mB + 1024, voffB, srdB, sB + 4 * LDB * 2);
    dma16<0>(mB + 2048, voffB, srdB, sB + 8 * LDB * 2);
    dma16<0>(mB + 3072, voffB, srdB, sB + 12 * LDB * 2);
  };
  const int m = lane & 31, h = lane >> 5;
  unsigned aaddr[4];
#pragma unroll
  for (int ks = 0; ks < 4; ++ks) aaddr[ks] = ldsbase + (unsigned)((wm * 64 + m) * 128 + (((2 * ks + h) ^ ((m >> 1) & 7)) << 4));
  const int g = lane >> 4, hh = g >> 1, ch = g & 1, q = (lane >> 2) & 3, p = lane & 3;
  unsigned baddr[2];
#pragma unroll
  for (int nb = 0; nb < 2; ++nb) baddr[nb] = ldsbase + 16384 + (unsigned)((8 * hh + q) * 256 + (((wn * 2 + nb) ^ q) << 6) + ch * 32 + p * 8);

#pragma unroll
  for (int i = 0; i < 2; ++i)
#pragma unroll
    for (int j = 0; j < 2; ++j) acc[i][j] = f32x16{};

  auto rdA = [&](unsigned stg, int ks, int mb) { return lds_b128(aaddr[ks] + stg + mb * 4096); };
  auto rdB = [&](unsigned stg, int ks, int nb) {
    return cat8(lds_tr(baddr[nb] + stg + (16 * ks) * 256), lds_tr(baddr[nb] + stg + (16 * ks + 4) * 256)); };

  issue(0, 0); issue(1, G_STAGE); issue(2, 2 * G_STAGE);
  asm volatile("s_waitcnt vmcnt(16)\n\ts_barrier" ::: "memory");
  bf16x8 af[2] = {rdA(0, 0, 0), rdA(0, 0, 1)}, bfr[2] = {rdB(0, 0, 0), rdB(0, 0, 1)};
  unsigned stg = 0;
  for (int kt = 0; kt < NT; ++kt) {
    if (kt + 2 < NT) asm volatile("s_waitcnt vmcnt(8)\n\ts_barrier" ::: "memory");
    else             asm volatile("s_waitcnt vmcnt(0)\n\ts_barrier" ::: "memory");
    if (kt + 3 < NT) issue(kt + 3, (stg + 3 * G_STAGE) & (G_NS * G_STAGE - 1));
    const unsigned nstg = (stg + G_STAGE) & (G_NS * G_STAGE - 1);
#pragma unroll
    for (int ks = 0; ks < 4; ++ks) {
      bf16x8 an[2], bn[2];
      if (ks < 3) { an[0] = rdA(stg, ks + 1, 0); an[1] = rdA(stg, ks + 1, 1); bn[0] = rdB(stg, ks + 1, 0); bn[1] = rdB(stg, ks + 1, 1); }
      else        { an[0] = rdA(nstg, 0, 0); an[1] = rdA(nstg, 0, 1); bn[0] = rdB(nstg, 0, 0); bn[1] = rdB(nstg, 0, 1); }
#pragma unroll
      for (int nb = 0; nb < 2; ++nb)
#pragma unroll
        for (int mb = 0; mb < 2; ++mb) acc[nb][mb] = mfma32(bfr[nb], af[mb], acc[nb][mb]);
      af[0] = an[0]; af[1] = an[1]; bfr[0] = bn[0]; bfr[1] = bn[1];
    }
    stg = nstg;
  }
}


__global__ __launch_bounds__(256, 1) void qproj_kernel(const __hip_bfloat16* __restrict__ x, const __hip_bfloat16* __restrict__ wq,
                                                       __hip_bfloat16* __restrict__ Qs) {
  __shared__ __attribute__((aligned(1024))) char lds[G_NS * G_STAGE];
  const unsigned ldsbase = (unsigned)(uintptr_t)LDSP(char, lds);
  const int bid = blockIdx.x, xcd = bid & 7, j = bid >> 3;
  const int nt = xcd * 4 + (j & 3), mt = j >> 2;
  f32x16 acc[2][2];
  gemm_tile<DM_, QCOLS, DM_>(x + (size_t)mt * 128 * DM_, (unsigned)((MROWS - mt * 128) * DM_ * 2),
                             wq + nt * 128, (unsigned)((DM_ * QCOLS - nt * 128) * 2), ldsbase, acc);
  const int lane = threadIdx.x & 63, wid = threadIdx.x >> 6, wm = wid >> 1, wn = wid & 1, m = lane & 31, h = lane >> 5;
  asm volatile("s_waitcnt lgkmcnt(0)\n\ts_barrier" ::: "memory");
#pragma unroll
  for (int nb = 0; nb < 2; ++nb)
#pragma unroll
    for (int mb = 0; mb < 2; ++mb)
#pragma unroll
      for (int rq = 0; rq < 4; ++rq) {
        u32x2 w = {cvtpk(acc[nb][mb][4 * rq] * QSCALE, acc[nb][mb][4 * rq + 1] * QSCALE), cvtpk(acc[nb][mb][4 * rq + 2] * QSCALE, acc[nb][mb][4 * rq + 3] * QSCALE)};
        *LDSP(u32x2, ldsbase + (wm * 64 + mb * 32 + m) * 272 + (wn * 64 + nb * 32 + 8 * rq + 4 * h) * 2) = w;
      }
  asm volatile("s_waitcnt lgkmcnt(0)\n\ts_barrier" ::: "memory");
  __hip_bfloat16* Cg = Qs + (size_t)mt * 128 * QCOLS + nt * 128;
#pragma unroll
  for (int it = 0; it < 8; ++it) {
    const int row = it * 16 + (threadIdx.x >> 4), ck = threadIdx.x & 15;
    u32x4 v = *LDSP(const u32x4, ldsbase + row * 272 + ck * 16);
    *reinterpret_cast<u32x4*>(Cg + (size_t)row * QCOLS + ck * 8) = v;
  }
}

__global__ __launch_bounds__(256, 1) void oproj_kernel(const __hip_bfloat16* __restrict__ O, const __hip_bfloat16* __restrict__ wo,
                                                       float* __restrict__ part) {
  __shared__ __attribute__((aligned(1024))) char lds[G_NS * G_STAGE];
  const unsigned ldsbase = (unsigned)(uintptr_t)LDSP(char, lds);
  const int bid = blockIdx.x, xcd = bid & 7, j = bid >> 3;
  const int ksp = xcd >> 1, mt = (xcd & 1) * 4 + (j & 3), nt = j >> 2;
  f32x16 acc[2][2];
  gemm_tile<QCOLS, DM_, 1024>(O + (size_t)mt * 128 * QCOLS + ksp * 1024, (unsigned)(((MROWS - mt * 128) * QCOLS - ksp * 1024) * 2),
                              wo + (size_t)ksp * 1024 * DM_ + nt * 128, (unsigned)(((QCOLS - ksp * 1024) * DM_ - nt * 128) * 2), ldsbase, acc);
  const int lane = threadIdx.x & 63, wid = threadIdx.x >> 6, wm = wid >> 1, wn = wid & 1, m = lane & 31, h = lane >> 5;
  float* P = part + (size_t)ksp * MROWS * DM_ + (size_t)(mt * 128) * DM_ + nt * 128;
#pragma unroll
  for (int nb = 0; nb < 2; ++nb)
#pragma unroll
    for (int mb = 0; mb < 2; ++mb)
#pragma unroll
      for (int rq = 0; rq < 4; ++rq) {
        f32x4 v = {acc[nb][mb][4 * rq], acc[nb][mb][4 * rq + 1], acc[nb][mb][4 * rq + 2], acc[nb][mb][4 * rq + 3]};
        *reinterpret_cast<f32x4*>(P + (size_t)(wm * 64 + mb * 32 + m) * DM_ + wn * 64 + nb * 32 + 8 * rq + 4 * h) = v;
      }
}

__global__ __launch_bounds__(256) void oreduce_kernel(const float* __restrict__ part, __hip_bfloat16* __restrict__ out) {
  const size_t i = ((size_t)blockIdx.x * 256 + threadIdx.x) * 8;
  f32x4 a0 = {}, a1 = {};
#pragma unroll
  for (int s = 0; s < 4; ++s) {
    a0 += *reinterpret_cast<const f32x4*>(part + (size_t)s * MROWS * DM_ + i);
    a1 += *reinterpret_cast<const f32x4*>(part + (size_t)s * MROWS * DM_ + i + 4);
  }
  u32x4 w = {cvtpk(a0[0], a0[1]), cvtpk(a0[2], a0[3]), cvtpk(a1[0], a1[1]), cvtpk(a1[2], a1[3])};
  *reinterpret_cast<u32x4*>(out + i) = w;
}

constexpr int A_KOFF = 0, A_VOFF = 65536, A_STG = 32768, A_LDS = 131072;
constexpr float A_THR = 8.0f;
constexpr int A_NIT = SK_ / 128;

struct ACtx {
  f32x16 O[4];
  bf16x8 Q[8];
  f32x16 S[2][2];
  bf16x8 P[4];
  f32x16 NEGM;
  bf16x8 F[4];
  float m, l0, l1;
  float mx0, mx1, alpha;
  unsigned kaddr[8], vaddr[4];
  unsigned voffK[4], voffV, m0K[2], m0V[2], soffbase;
  __amdgpu_buffer_rsrc_t srdK, srdV;
};

template <int T, int KS, int VS> DI void a_read(ACtx& c) {
  if constexpr (T < 16) {
    constexpr int kb = T >> 3, step = T & 7;
    c.F[T & 3] = lds_b128(c.kaddr[step] + KS * A_STG + kb * 8192);
  } else {
    constexpr int u = T - 16, ks = u >> 2, db = u & 3;
    c.F[T & 3] = cat8(lds_tr(c.vaddr[db] + VS * A_STG + ks * 4096), lds_tr(c.vaddr[db] + VS * A_STG + ks * 4096 + 2048));
  }
}

template <int X, int D> DI void a_dma(ACtx& c, int it) {
  if constexpr (D < 4) dma16<(D & 3) * 1024>(c.m0K[X ^ 1], c.voffK[D & 3], c.srdK, c.soffbase + (unsigned)(it + 1) * 32768u);
  else                 dma16<(D & 3) * 1024>(c.m0V[X],     c.voffV,        c.srdV, c.soffbase + (unsigned)it * 32768u);
}

template <int XP, int G> DI void a_part2(ACtx& c) {
  if constexpr (G < 8) {
    c.S[XP][1][2 * G]     = __builtin_amdgcn_exp2f(c.S[XP][1][2 * G]);
    c.S[XP][1][2 * G + 1] = __builtin_amdgcn_exp2f(c.S[XP][1][2 * G + 1]);
  } else {
    constexpr int u = G - 8;
    c.l0 += c.S[XP][1][2 * u]; c.l1 += c.S[XP][1][2 * u + 1];
#pragma unroll
    for (int e = 0; e < 2; ++e) {
      constexpr int dummy = 0; (void)dummy;
      const int Dw = 2 * u + e, ks = Dw >> 2, pd = Dw & 3;
      u32x4 w = __builtin_bit_cast(u32x4, c.P[ks]);
      w[pd] = cvtpk(c.S[XP][ks >> 1][8 * (ks & 1) + 2 * pd], c.S[XP][ks >> 1][8 * (ks & 1) + 2 * pd + 1]);
      c.P[ks] = __builtin_bit_cast(bf16x8, w);
    }
  }
}

template <int X, int G, bool FIRST> DI void a_part1(ACtx& c, bool& pend) {
  f32x16 (&S)[2] = c.S[X];
  if constexpr (G < 4) {
    constexpr int kb = G >> 1, r = 8 * (G & 1);
    if constexpr (G == 0) { c.mx0 = max3f(S[0][0], S[0][1], S[0][2]); c.mx1 = max3f(S[0][3], S[0][4], S[0][5]); c.mx0 = max3f(c.mx0, S[0][6], S[0][7]); }
    else { c.mx0 = max3f(c.mx0, S[kb][r], S[kb][r + 1]); c.mx1 = max3f(c.mx1, S[kb][r + 2], S[kb][r + 3]);
           c.mx0 = max3f(c.mx0, S[kb][r + 4], S[kb][r + 5]); c.mx1 = max3f(c.mx1, S[kb][r + 6], S[kb][r + 7]); }
  } else if constexpr (G == 4) {
    float mm = max2f(c.mx0, c.mx1);
    auto pr = __builtin_amdgcn_permlane32_swap(__float_as_uint(mm), __float_as_uint(mm), false, false);
    const float rmax = max2f(__uint_as_float(pr[0]), __uint_as_float(pr[1]));
    if constexpr (FIRST) {
      c.m = rmax;
#pragma unroll
      for (int r = 0; r < 16; ++r) { S[0][r] -= rmax; S[1][r] -= rmax; c.NEGM[r] = -rmax; }
    } else {
      if (__builtin_expect(__any(rmax > A_THR), 0)) {
        const float delta = fmaxf(rmax, 0.f);
        c.alpha = __builtin_amdgcn_exp2f(-delta);
        c.m += delta; c.l0 *= c.alpha; c.l1 *= c.alpha;
#pragma unroll
        for (int r = 0; r < 16; ++r) { S[0][r] -= delta; S[1][r] -= delta; c.NEGM[r] = -c.m; }
        pend = true;
      }
    }
  } else {
    constexpr int u = G - 5;
    constexpr int e0 = (u * 16) / 11, e1 = ((u + 1) * 16) / 11;
#pragma unroll
    for (int e = e0; e < e1; ++e) S[0][e] = __builtin_amdgcn_exp2f(S[0][e]);
    constexpr int a0 = u == 0 ? 0 : ((u - 1) * 16) / 11, a1 = u == 0 ? 0 : e0;
#pragma unroll
    for (int e = a0; e < a1; ++e) { if (e & 1) c.l1 += S[0][e]; else c.l0 += S[0][e]; }
    if constexpr (G == 15) {
#pragma unroll
      for (int e = e0; e < 16; ++e) { if (e & 1) c.l1 += S[0][e]; else c.l0 += S[0][e]; }
    }
  }
}

template <int X, bool QK, bool PV, bool FIRST, bool DK, bool DV, bool NEXTK>
DI void a_iter(ACtx& c, int it) {
  bool pend = false;
  static_for<0, 32>([&](auto SS) {
    constexpr int s = decltype(SS)::value;
    if constexpr (s == 29) {
      asm volatile("s_waitcnt vmcnt(0) lgkmcnt(0)\n\ts_barrier" ::: "memory");
    }
    if constexpr (s < 16) {
      if constexpr (QK) {
        constexpr int kb = s >> 3, step = s & 7;
        if constexpr (step == 0) {
          if constexpr (FIRST) c.S[X][kb] = mfma32(c.F[s & 3], c.Q[0], f32x16{});
          else                 c.S[X][kb] = mfma32(c.F[s & 3], c.Q[0], c.NEGM);
        } else c.S[X][kb] = mfma32(c.F[s & 3], c.Q[step], c.S[X][kb]);
      }
    } else {
      if constexpr (PV) { constexpr int u = s - 16, ks = u >> 2, db = u & 3; c.O[db] = mfma32(c.F[s & 3], c.P[ks], c.O[db]); }
    }
    constexpr int t = s + 3;
    if constexpr (t < 16) { if constexpr (QK) a_read<t, X, X ^ 1>(c); }
    else if constexpr (t < 32) { if constexpr (PV) a_read<t, X, X ^ 1>(c); }
    else { if constexpr (NEXTK) a_read<t - 32, X ^ 1, X>(c); }
    if constexpr (s < 16 && (s & 1) == 0) {
      constexpr int D = s >> 1;
      if constexpr (D < 4) { if constexpr (DK) a_dma<X, D>(c, it); }
      else                 { if constexpr (DV) a_dma<X, D>(c, it); }
    }
    if constexpr (s < 16) { if constexpr (!FIRST) a_part2<X ^ 1, s>(c); }
    else { if constexpr (QK) a_part1<X, s - 16, FIRST>(c, pend); }
    SBAR();
  });
  if constexpr (QK && !FIRST && PV) {
    if (__builtin_expect(pend, 0)) {
#pragma unroll
      for (int d = 0; d < 4; ++d)
#pragma unroll
        for (int r = 0; r < 16; ++r) c.O[d][r] *= c.alpha;
    }
  }
}

__global__ __launch_bounds__(512, 2) void attn_kernel(const __hip_bfloat16* __restrict__ Qs, const __hip_bfloat16* __restrict__ kc,
                                                      const __hip_bfloat16* __restrict__ vc, __hip_bfloat16* __restrict__ Og) {
  __shared__ __attribute__((aligned(1024))) char lds[A_LDS];
  const unsigned ldsbase = (unsigned)(uintptr_t)LDSP(char, lds);
  const int bid = blockIdx.x, xcd = bid & 7, j = bid >> 3;
  const int pair = j >> 1, half = j & 1, hkv = xcd & 3, b = (xcd >> 2) * 16 + pair;
  const int tid = threadIdx.x, lane = tid & 63;
  const int wid = __builtin_amdgcn_readfirstlane(tid >> 6);
  const int rb = wid & 3, kh = wid >> 2;
  const int head = hkv * 8 + half * 4 + rb;
  ACtx c;
  const size_t kvoff = (size_t)(b * HKV_ + hkv) * SK_ * DH_;
  c.srdK = __builtin_amdgcn_make_buffer_rsrc((void*)(kc + kvoff), 0, SK_ * DH_ * 2, 0x00020000);
  c.srdV = __builtin_amdgcn_make_buffer_rsrc((void*)(vc + kvoff), 0, SK_ * DH_ * 2, 0x00020000);
  {
    const int jr = lane >> 4, sl = lane & 15;
#pragma unroll
    for (int k = 0; k < 4; ++k) c.voffK[k] = (unsigned)(jr * 256 + ((sl ^ ((4 * k + jr) & 15)) << 4));
    c.voffV = (unsigned)(jr * 256 + ((sl ^ (jr << 2)) << 4));
#pragma unroll
    for (int s = 0; s < 2; ++s) {
      c.m0K[s] = ldsbase + A_KOFF + s * A_STG + kh * 16384 + rb * 4096;
      c.m0V[s] = ldsbase + A_VOFF + s * A_STG + kh * 16384 + rb * 4096;
    }
    c.soffbase = (unsigned)(kh * 16384 + rb * 4096);
    const int kr = lane & 31, hi = lane >> 5;
#pragma unroll
    for (int st = 0; st < 8; ++st) c.kaddr[st] = ldsbase + A_KOFF + (unsigned)(kh * 16384 + kr * 256 + (((2 * st + hi) ^ (kr & 15)) << 4));
    const int g = lane >> 4, hh = g >> 1, ch = g & 1, q = (lane >> 2) & 3, p = lane & 3;
#pragma unroll
    for (int db = 0; db < 4; ++db) c.vaddr[db] = ldsbase + A_VOFF + (unsigned)(kh * 16384 + (4 * hh + q) * 256 + ((db ^ q) << 6) + ch * 32 + p * 8);
  }
#pragma unroll
  for (int k = 0; k < 4; ++k) {
    if (k == 0) dma16<0>(c.m0K[0], c.voffK[0], c.srdK, c.soffbase);
    if (k == 1) dma16<1024>(c.m0K[0], c.voffK[1], c.srdK, c.soffbase);
    if (k == 2) dma16<2048>(c.m0K[0], c.voffK[2], c.srdK, c.soffbase);
    if (k == 3) dma16<3072>(c.m0K[0], c.voffK[3], c.srdK, c.soffbase);
  }
  dma16<0>(c.m0K[1], c.voffK[0], c.srdK, c.soffbase + 32768u);
  dma16<1024>(c.m0K[1], c.voffK[1], c.srdK, c.soffbase + 32768u);
  dma16<2048>(c.m0K[1], c.voffK[2], c.srdK, c.soffbase + 32768u);
  dma16<3072>(c.m0K[1], c.voffK[3], c.srdK, c.soffbase + 32768u);
  dma16<0>(c.m0V[0], c.voffV, c.srdV, c.soffbase);
  dma16<1024>(c.m0V[0], c.voffV, c.srdV, c.soffbase);
  dma16<2048>(c.m0V[0], c.voffV, c.srdV, c.soffbase);
  dma16<3072>(c.m0V[0], c.voffV, c.srdV, c.soffbase);
  {
    const bf16x8* qp = reinterpret_cast<const bf16x8*>(Qs + (size_t)(b * SQ_ + (lane & 31)) * QCOLS + head * DH_ + (lane >> 5) * 8);
#pragma unroll
    for (int st = 0; st < 8; ++st) c.Q[st] = qp[2 * st];
  }
#pragma unroll
  for (int d = 0; d < 4; ++d) c.O[d] = f32x16{};
  c.l0 = 0.f; c.l1 = 0.f; c.m = 0.f; c.alpha = 1.f;
  asm volatile("s_waitcnt vmcnt(0)\n\ts_barrier" ::: "memory");
  a_read<0, 0, 1>(c); a_read<1, 0, 1>(c); a_read<2, 0, 1>(c);
  a_iter<0, true, false, true, false, false, true>(c, 0);
  for (int it = 1; it + 1 < A_NIT - 1; it += 2) {
    a_iter<1, true, true, false, true, true, true>(c, it);
    a_iter<0, true, true, false, true, true, true>(c, it + 1);
  }
  a_iter<1, true, true, false, false, true, false>(c, A_NIT - 1);
  a_iter<0, false, true, false, false, false, false>(c, A_NIT);

  float l = c.l0 + c.l1;
  { auto pr = __builtin_amdgcn_permlane32_swap(__float_as_uint(l), __float_as_uint(l), false, false);
    l = __uint_as_float(pr[0]) + __uint_as_float(pr[1]); }
  asm volatile("s_waitcnt lgkmcnt(0)\n\ts_barrier" ::: "memory");
  const unsigned xbase = ldsbase + rb * 17408;
  if (kh == 1) {
#pragma unroll
    for (int d = 0; d < 4; ++d)
#pragma unroll
      for (int rq = 0; rq < 4; ++rq) {
        f32x4 v = {c.O[d][4 * rq], c.O[d][4 * rq + 1], c.O[d][4 * rq + 2], c.O[d][4 * rq + 3]};
        *LDSP(f32x4, xbase + (d * 4 + rq) * 1024 + lane * 16) = v;
      }
    f32x2 ml = {c.m, l};
    *LDSP(f32x2, xbase + 16384 + lane * 8) = ml;
  }
  asm volatile("s_waitcnt lgkmcnt(0)\n\ts_barrier" ::: "memory");
  if (kh == 0) {
    const f32x2 ml = *LDSP(const f32x2, xbase + 16384 + lane * 8);
    const float mnew = fmaxf(c.m, ml[0]);
    const float f0 = __builtin_amdgcn_exp2f(c.m - mnew), f1 = __builtin_amdgcn_exp2f(ml[0] - mnew);
    const float inv = 1.0f / (f0 * l + f1 * ml[1]);
    const float g0 = f0 * inv, g1 = f1 * inv;
    const int qrow = lane & 31, hi = lane >> 5;
#pragma unroll
    for (int d = 0; d < 4; ++d)
#pragma unroll
      for (int rq = 0; rq < 4; ++rq) {
        const f32x4 v = *LDSP(const f32x4, xbase + (d * 4 + rq) * 1024 + lane * 16);
        const float o0 = c.O[d][4 * rq] * g0 + v[0] * g1, o1 = c.O[d][4 * rq + 1] * g0 + v[1] * g1;
        const float o2 = c.O[d][4 * rq + 2] * g0 + v[2] * g1, o3 = c.O[d][4 * rq + 3] * g0 + v[3] * g1;
        u32x2 w = {cvtpk(o0, o1), cvtpk(o2, o3)};
        c.O[d][4 * rq] = __uint_as_float(w[0]); c.O[d][4 * rq + 1] = __uint_as_float(w[1]);
      }
    asm volatile("s_waitcnt lgkmcnt(0)" ::: "memory");
#pragma unroll
    for (int d = 0; d < 4; ++d)
#pragma unroll
      for (int rq = 0; rq < 4; ++rq) {
        u32x2 w = {__float_as_uint(c.O[d][4 * rq]), __float_as_uint(c.O[d][4 * rq + 1])};
        *LDSP(u32x2, xbase + qrow * 272 + (32 * d + 8 * rq + 4 * hi) * 2) = w;
      }
    asm volatile("s_waitcnt lgkmcnt(0)" ::: "memory");
    __hip_bfloat16* og = Og + (size_t)(b * SQ_) * QCOLS + head * DH_;
#pragma unroll
    for (int it = 0; it < 8; ++it) {
      const int row = it * 4 + (lane >> 4), ck = lane & 15;
      u32x4 v = *LDSP(const u32x4, xbase + row * 272 + ck * 16);
      *reinterpret_cast<u32x4*>(og + (size_t)row * QCOLS + ck * 8) = v;
    }
  }
}

extern "C" void kernel_launch(void* const* d_in, const int* in_sizes, int n_in, void* d_out, int out_size, void* d_ws, size_t ws_size,
                              hipStream_t stream) {
  const __hip_bfloat16* x = (const __hip_bfloat16*)d_in[0];
  const __hip_bfloat16* kc = (const __hip_bfloat16*)d_in[1];
  const __hip_bfloat16* vc = (const __hip_bfloat16*)d_in[2];
  const __hip_bfloat16* wq = (const __hip_bfloat16*)d_in[3];
  const __hip_bfloat16* wo = (const __hip_bfloat16*)d_in[4];
  char* ws = (char*)d_ws;
  __hip_bfloat16* Qs = (__hip_bfloat16*)ws;
  __hip_bfloat16* Ob = (__hip_bfloat16*)(ws + (size_t)8 * 1024 * 1024);
  float* part = (float*)(ws + (size_t)16 * 1024 * 1024);
  qproj_kernel<<<256, 256, 0, stream>>>(x, wq, Qs);
  attn_kernel<<<256, 512, 0, stream>>>(Qs, kc, vc, Ob);
  oproj_kernel<<<256, 256, 0, stream>>>(Ob, wo, part);
  oreduce_kernel<<<MROWS * DM_ / 8 / 256, 256, 0, stream>>>(part, (__hip_bfloat16*)d_out);
}
```

```cpp
#include <hip/hip_runtime.h>
#include <hip/hip_bf16.h>
#include <stdint.h>
#define STAMP(K, I)
#define A_ABL 0
#define G_ABL 0

typedef __attribute__((ext_vector_type(8))) __bf16 bf16x8;
typedef __attribute__((ext_vector_type(4))) __bf16 bf16x4;
typedef __attribute__((ext_vector_type(2))) __bf16 bf16x2;
typedef __attribute__((ext_vector_type(16))) float f32x16;
typedef __attribute__((ext_vector_type(4))) float f32x4;
typedef __attribute__((ext_vector_type(2))) float f32x2;
typedef __attribute__((ext_vector_type(4))) unsigned u32x4;
typedef __attribute__((ext_vector_type(2))) unsigned u32x2;

#define DI __device__ __forceinline__
#define LDSP(T, a) ((__attribute__((address_space(3))) T*)(uintptr_t)(a))
#define SBAR() __builtin_amdgcn_sched_barrier(0)

constexpr int NB_ = 32, SQ_ = 32, DM_ = 1024, HQ_ = 32, HKV_ = 4, SK_ = 2048, DH_ = 128;
constexpr int MROWS = NB_ * SQ_;
constexpr int QCOLS = HQ_ * DH_;
constexpr float QSCALE = 0.08838834764831845f * 1.4426950408889634f;

DI unsigned cvtpk(float lo, float hi) {
  f32x2 v = {lo, hi}; bf16x2 b = __builtin_convertvector(v, bf16x2); return __builtin_bit_cast(unsigned, b);
}
DI float max3f(float a, float b, float c) { float r; asm("v_max3_f32 %0, %1, %2, %3" : "=v"(r) : "v"(a), "v"(b), "v"(c)); return r; }
DI float max2f(float a, float b) { float r; asm("v_max_f32_e32 %0, %1, %2" : "=v"(r) : "v"(a), "v"(b)); return r; }
#define PIN(x) asm volatile("" : "+v"(x))
DI f32x16 mfma32(bf16x8 a, bf16x8 b, f32x16 c) { return __builtin_amdgcn_mfma_f32_32x32x16_bf16(a, b, c, 0, 0, 0); }

template <int IMM, int POL = 0> DI void dma16(unsigned m0v, unsigned voff, __amdgpu_buffer_rsrc_t srd, unsigned soff) {
  if constexpr (POL == 0)
    asm volatile("s_mov_b32 m0, %0\n\ts_nop 0\n\tbuffer_load_dwordx4 %1, %2, %3 offen offset:%4 lds"
                 :: "s"(m0v), "v"(voff), "s"(srd), "s"(soff), "i"(IMM) : "memory");
  else
    asm volatile("s_mov_b32 m0, %0\n\ts_nop 0\n\tbuffer_load_dwordx4 %1, %2, %3 offen offset:%4 nt lds"
                 :: "s"(m0v), "v"(voff), "s"(srd), "s"(soff), "i"(IMM) : "memory");
}
#define KV_POL 1
#define KV_POL2 1
#define GA_POL 0
#define GB_POL 0
DI bf16x8 lds_b128(unsigned a) { return *LDSP(const bf16x8, a); }
DI bf16x4 lds_tr(unsigned a) { return __builtin_amdgcn_ds_read_tr16_b64_v4bf16(LDSP(bf16x4, a)); }
DI bf16x8 cat8(bf16x4 a, bf16x4 b) { return __builtin_shufflevector(a, b, 0, 1, 2, 3, 4, 5, 6, 7); }

template <int I> struct IC { static constexpr int value = I; };
template <int B, int E, class F> DI void static_for(F&& f) {
  if constexpr (B < E) { f(IC<B>{}); static_for<B + 1, E>(static_cast<F&&>(f)); }
}

constexpr int G_NS = 4;
constexpr int G_STAGE = 32768;

template <int LDA, int LDB, int KLEN>
DI void gemm_tile(const __hip_bfloat16* __restrict__ Ag, unsigned Abytes, const __hip_bfloat16* __restrict__ Bg, unsigned Bbytes,
                  unsigned ldsbase, int rot, f32x16 (&acc)[2][2]) {
  constexpr int NT = KLEN / 64;
  static_assert((NT & (NT - 1)) == 0 && NT >= 4, "power-of-two tile count");
  const int tid = threadIdx.x, lane = tid & 63;
  const int wid = __builtin_amdgcn_readfirstlane(tid >> 6);
  if (wid >= 4) {
    const int lw = wid - 4;
    auto srdA = __builtin_amdgcn_make_buffer_rsrc((void*)Ag, 0, Abytes, 0x00020000);
    auto srdB = __builtin_amdgcn_make_buffer_rsrc((void*)Bg, 0, Bbytes, 0x00020000);
    const unsigned voffA0 = (unsigned)((lane >> 3) * LDA * 2 + ((((lane & 7) ^ ((lane >> 4) & 7))) << 4));
    const unsigned voffA1 = (unsigned)((lane >> 3) * LDA * 2 + ((((lane & 7) ^ ((4 + (lane >> 4)) & 7))) << 4));
    const unsigned voffB = (unsigned)((lane >> 4) * LDB * 2 + (((lane & 15) ^ (((lane >> 4) & 3) << 2)) << 4));
    auto issue = [&](int kt) {
      const int kr = (kt + rot) & (NT - 1);
      const unsigned mA = ldsbase + (unsigned)(kt & (G_NS - 1)) * G_STAGE + lw * 4096, mB = mA + 16384;
      const unsigned sA = (unsigned)(kr * 128 + lw * 4 * 16 * LDA), sB = (unsigned)((kr * 64 + lw * 16) * LDB * 2);
      dma16<0, GA_POL>(mA, voffA0, srdA, sA);
      dma16<0, GB_POL>(mB, voffB, srdB, sB);
      dma16<0, GA_POL>(mA + 1024, voffA1, srdA, sA + 16 * LDA);
      dma16<0, GB_POL>(mB + 1024, voffB, srdB, sB + 4 * LDB * 2);
      dma16<0, GA_POL>(mA + 2048, voffA0, srdA, sA + 32 * LDA);
      dma16<0, GB_POL>(mB + 2048, voffB, srdB, sB + 8 * LDB * 2);
      dma16<0, GA_POL>(mA + 3072, voffA1, srdA, sA + 48 * LDA);
      dma16<0, GB_POL>(mB + 3072, voffB, srdB, sB + 12 * LDB * 2);
    };
    issue(0); issue(1); issue(2);
    asm volatile("s_waitcnt vmcnt(16)\n\ts_barrier" ::: "memory");
    for (int kt = 0; kt < NT; ++kt) {
      if (kt + 2 < NT) asm volatile("s_waitcnt vmcnt(8)\n\ts_barrier" ::: "memory");
      else             asm volatile("s_waitcnt vmcnt(0)\n\ts_barrier" ::: "memory");
      if (kt + 3 < NT) issue(kt + 3);
    }
    return;
  }
  const int wm = wid >> 1, wn = wid & 1;
  const int m = lane & 31, h = lane >> 5;
  unsigned aaddr[4];
#pragma unroll
  for (int ks = 0; ks < 4; ++ks) aaddr[ks] = ldsbase + (unsigned)((wm * 64 + m) * 128 + (((2 * ks + h) ^ ((m >> 1) & 7)) << 4));
  const int g = lane >> 4, hh = g >> 1, ch = g & 1, q = (lane >> 2) & 3, p = lane & 3;
  unsigned baddr[2];
#pragma unroll
  for (int nb = 0; nb < 2; ++nb) baddr[nb] = ldsbase + 16384 + (unsigned)((8 * hh + q) * 256 + (((wn * 2 + nb) ^ q) << 6) + ch * 32 + p * 8);
#pragma unroll
  for (int i = 0; i < 2; ++i)
#pragma unroll
    for (int j = 0; j < 2; ++j) acc[i][j] = f32x16{};
  auto rdA = [&](unsigned stg, int ks, int mb) { return lds_b128(aaddr[ks] + stg + mb * 4096); };
  auto rdB = [&](unsigned stg, int ks, int nb) {
    return cat8(lds_tr(baddr[nb] + stg + (16 * ks) * 256), lds_tr(baddr[nb] + stg + (16 * ks + 4) * 256)); };
  asm volatile("s_barrier" ::: "memory");
  STAMP((LDA == DM_ ? 0 : 1), 1);
  bf16x8 af[2][2], bfr[2][2];
  af[0][0] = rdA(0, 0, 0); af[0][1] = rdA(0, 0, 1); bfr[0][0] = rdB(0, 0, 0); bfr[0][1] = rdB(0, 0, 1);
  unsigned stg = 0;
  for (int kt = 0; kt < NT; ++kt) {
    asm volatile("s_barrier" ::: "memory");
    const unsigned nstg = (stg + G_STAGE) & (G_NS * G_STAGE - 1);
    static_for<0, 16>([&](auto SS) {
      constexpr int s = decltype(SS)::value, ks = s >> 2, sub = s & 3, cur = ks & 1, nxt = cur ^ 1;
      constexpr int nb = sub >> 1, mb = sub & 1;
      acc[nb][mb] = mfma32(bfr[cur][nb], af[cur][mb], acc[nb][mb]);
      if constexpr (sub < 2) {
        if constexpr (ks < 3) { af[nxt][sub] = rdA(stg, ks + 1, sub); bfr[nxt][sub] = rdB(stg, ks + 1, sub); }
        else                  { af[nxt][sub] = rdA(nstg, 0, sub);     bfr[nxt][sub] = rdB(nstg, 0, sub); }
      }
      SBAR();
    });
    stg = nstg;
  }
}

__global__ __launch_bounds__(512, 2) void qproj_kernel(const __hip_bfloat16* __restrict__ x, const __hip_bfloat16* __restrict__ wq,
                                                       __hip_bfloat16* __restrict__ Qs) {
  __shared__ __attribute__((aligned(1024))) char lds[G_NS * G_STAGE];
  const unsigned ldsbase = (unsigned)(uintptr_t)LDSP(char, lds);
  const int bid = blockIdx.x, xcd = bid & 7, j = bid >> 3;
  const int nt = xcd * 4 + (j & 3), mt = j >> 2;
  STAMP(0, 0);
  f32x16 acc[2][2];
  gemm_tile<DM_, QCOLS, DM_>(x + (size_t)mt * 128 * DM_, (unsigned)((MROWS - mt * 128) * DM_ * 2),
                             wq + nt * 128, (unsigned)((DM_ * QCOLS - nt * 128) * 2), ldsbase, (2 * mt + 5 * (j & 3)) & 15, acc);
  STAMP(0, 2);
  const int lane = threadIdx.x & 63, wid = threadIdx.x >> 6, wm = wid >> 1, wn = wid & 1, m = lane & 31, h = lane >> 5;
  asm volatile("s_waitcnt lgkmcnt(0)\n\ts_barrier" ::: "memory");
  if (wid < 4) {
#pragma unroll
    for (int nb = 0; nb < 2; ++nb)
#pragma unroll
      for (int mb = 0; mb < 2; ++mb)
#pragma unroll
        for (int rq = 0; rq < 4; ++rq) {
          u32x2 w = {cvtpk(acc[nb][mb][4 * rq] * QSCALE, acc[nb][mb][4 * rq + 1] * QSCALE), cvtpk(acc[nb][mb][4 * rq + 2] * QSCALE, acc[nb][mb][4 * rq + 3] * QSCALE)};
          *LDSP(u32x2, ldsbase + (wm * 64 + mb * 32 + m) * 272 + (wn * 64 + nb * 32 + 8 * rq + 4 * h) * 2) = w;
        }
  }
  asm volatile("s_waitcnt lgkmcnt(0)\n\ts_barrier" ::: "memory");
  __hip_bfloat16* Cg = Qs + (size_t)mt * 128 * QCOLS + nt * 128;
#pragma unroll
  for (int it = 0; it < 4; ++it) {
    const int row = it * 32 + (threadIdx.x >> 4), ck = threadIdx.x & 15;
    u32x4 v = *LDSP(const u32x4, ldsbase + row * 272 + ck * 16);
    *reinterpret_cast<u32x4*>(Cg + (size_t)row * QCOLS + ck * 8) = v;
  }
  STAMP(0, 3);
}

__global__ __launch_bounds__(512, 2) void oproj_kernel(const __hip_bfloat16* __restrict__ O, const __hip_bfloat16* __restrict__ wo,
                                                       float* __restrict__ part) {
  __shared__ __attribute__((aligned(1024))) char lds[G_NS * G_STAGE];
  const unsigned ldsbase = (unsigned)(uintptr_t)LDSP(char, lds);
  const int bid = blockIdx.x, xcd = bid & 7, j = bid >> 3;
  const int ksp = xcd >> 1, mt = (xcd & 1) * 4 + (j & 3), nt = j >> 2;
  STAMP(1, 0);
  f32x16 acc[2][2];
  gemm_tile<QCOLS, DM_, 1024>(O + (size_t)mt * 128 * QCOLS + ksp * 1024, (unsigned)(((MROWS - mt * 128) * QCOLS - ksp * 1024) * 2),
                              wo + (size_t)ksp * 1024 * DM_ + nt * 128, (unsigned)(((QCOLS - ksp * 1024) * DM_ - nt * 128) * 2), ldsbase,
                              (2 * nt + 5 * (j & 3)) & 15, acc);
  STAMP(1, 2);
  const int lane = threadIdx.x & 63, wid = threadIdx.x >> 6, wm = wid >> 1, wn = wid & 1, m = lane & 31, h = lane >> 5;
  if (wid < 4) {
    float* P = part + (size_t)ksp * MROWS * DM_ + (size_t)(mt * 128) * DM_ + nt * 128;
#pragma unroll
    for (int nb = 0; nb < 2; ++nb)
#pragma unroll
      for (int mb = 0; mb < 2; ++mb)
#pragma unroll
        for (int rq = 0; rq < 4; ++rq) {
          f32x4 v = {acc[nb][mb][4 * rq], acc[nb][mb][4 * rq + 1], acc[nb][mb][4 * rq + 2], acc[nb][mb][4 * rq + 3]};
          *reinterpret_cast<f32x4*>(P + (size_t)(wm * 64 + mb * 32 + m) * DM_ + wn * 64 + nb * 32 + 8 * rq + 4 * h) = v;
        }
  }
  STAMP(1, 3);
}

__global__ __launch_bounds__(256) void oreduce_kernel(const float* __restrict__ part, __hip_bfloat16* __restrict__ out) {
  const size_t i = ((size_t)blockIdx.x * 256 + threadIdx.x) * 8;
  f32x4 a0 = {}, a1 = {};
#pragma unroll
  for (int s = 0; s < 4; ++s) {
    a0 += *reinterpret_cast<const f32x4*>(part + (size_t)s * MROWS * DM_ + i);
    a1 += *reinterpret_cast<const f32x4*>(part + (size_t)s * MROWS * DM_ + i + 4);
  }
  u32x4 w = {cvtpk(a0[0], a0[1]), cvtpk(a0[2], a0[3]), cvtpk(a1[0], a1[1]), cvtpk(a1[2], a1[3])};
  *reinterpret_cast<u32x4*>(out + i) = w;
}

constexpr int A_KOFF = 0, A_VOFF = 65536, A_STG = 32768, A_LDS = 131072;
constexpr float A_THR = 8.0f;
constexpr int A_NIT = SK_ / 128;

struct ACtx {
  f32x16 O[4];
  bf16x8 Q[8];
  f32x16 S[2][2];
  bf16x8 P[4];
  f32x16 NEGM;
  bf16x8 F[4];
  float m, l0, l1;
  float mx0, mx1, alpha;
  unsigned kaddr[8], vaddr[4];
  unsigned voffK[4], voffV, m0K[2], m0V[2], soffbase;
  unsigned voffR[2][4], m0R[2][2], tadd[2];
  __amdgpu_buffer_rsrc_t srdR[2];
  bool kfirst;
  __amdgpu_buffer_rsrc_t srdK, srdV;
};

template <int T, int KS, int VS> DI void a_read(ACtx& c) {
  if constexpr (T < 16) {
    constexpr int kb = T >> 3, step = T & 7;
    c.F[T & 3] = lds_b128(c.kaddr[step] + KS * A_STG + kb * 8192);
  } else {
    constexpr int u = T - 16, ks = u >> 2, db = u & 3;
    c.F[T & 3] = cat8(lds_tr(c.vaddr[db] + VS * A_STG + ks * 4096), lds_tr(c.vaddr[db] + VS * A_STG + ks * 4096 + 2048));
  }
}

template <int X, int P, bool DK, bool DV> DI void a_dma(ACtx& c, int it) {
  constexpr int R = P >> 2, D = P & 3;
  if constexpr (DK && DV) {
    dma16<D * 1024, (R == 0 ? KV_POL : KV_POL2)>(c.m0R[R][X], c.voffR[R][D], c.srdR[R], c.soffbase + ((unsigned)it + c.tadd[R]) * 32768u);
  } else if constexpr (DK || DV) {
    if ((R == 0) == (c.kfirst == DK))
      dma16<D * 1024, (R == 0 ? KV_POL : KV_POL2)>(c.m0R[R][X], c.voffR[R][D], c.srdR[R], c.soffbase + ((unsigned)it + c.tadd[R]) * 32768u);
  }
}

template <int XP, int G> DI void a_part2(ACtx& c) {
  if constexpr (G < 8) {
    float e0 = __builtin_amdgcn_exp2f(c.S[XP][1][2 * G]);     PIN(e0); c.S[XP][1][2 * G] = e0;
    float e1 = __builtin_amdgcn_exp2f(c.S[XP][1][2 * G + 1]); PIN(e1); c.S[XP][1][2 * G + 1] = e1;
  } else {
    constexpr int u = G - 8;
    c.l0 += c.S[XP][1][2 * u]; PIN(c.l0);
    c.l1 += c.S[XP][1][2 * u + 1]; PIN(c.l1);
    static_for<0, 2>([&](auto EE) {
      constexpr int Dw = 2 * u + decltype(EE)::value, ks = Dw >> 2, pd = Dw & 3;
      u32x4 w = __builtin_bit_cast(u32x4, c.P[ks]);
      unsigned pk = cvtpk(c.S[XP][ks >> 1][8 * (ks & 1) + 2 * pd], c.S[XP][ks >> 1][8 * (ks & 1) + 2 * pd + 1]); PIN(pk);
      w[pd] = pk;
      c.P[ks] = __builtin_bit_cast(bf16x8, w);
    });
  }
}

template <int X, int G, bool FIRST> DI void a_part1(ACtx& c, bool& pend) {
  f32x16 (&S)[2] = c.S[X];
  if constexpr (G < 4) {
    constexpr int kb = G >> 1, r = 8 * (G & 1);
    if constexpr (G == 0)
      asm volatile("v_max3_f32 %0, %2, %3, %4\n\tv_max3_f32 %1, %5, %6, %7\n\tv_max3_f32 %0, %0, %8, %9"
                   : "=&v"(c.mx0), "=&v"(c.mx1) : "v"(S[0][0]), "v"(S[0][1]), "v"(S[0][2]), "v"(S[0][3]), "v"(S[0][4]), "v"(S[0][5]), "v"(S[0][6]), "v"(S[0][7]));
    else
      asm volatile("v_max3_f32 %0, %0, %2, %3\n\tv_max3_f32 %1, %1, %4, %5\n\tv_max3_f32 %0, %0, %6, %7\n\tv_max3_f32 %1, %1, %8, %9"
                   : "+v"(c.mx0), "+v"(c.mx1) : "v"(S[kb][r]), "v"(S[kb][r + 1]), "v"(S[kb][r + 2]), "v"(S[kb][r + 3]), "v"(S[kb][r + 4]), "v"(S[kb][r + 5]), "v"(S[kb][r + 6]), "v"(S[kb][r + 7]));
  } else if constexpr (G == 4) {
    float mm = max2f(c.mx0, c.mx1);
    auto pr = __builtin_amdgcn_permlane32_swap(__float_as_uint(mm), __float_as_uint(mm), false, false);
    const float rmax = max2f(__uint_as_float(pr[0]), __uint_as_float(pr[1]));
    if constexpr (FIRST) {
      c.m = rmax;
#pragma unroll
      for (int r = 0; r < 16; ++r) { S[0][r] -= rmax; S[1][r] -= rmax; c.NEGM[r] = -rmax; }
    } else {
      if (__builtin_expect(__any(rmax > A_THR), 0)) {
        const float delta = fmaxf(rmax, 0.f);
        c.alpha = __builtin_amdgcn_exp2f(-delta);
        c.m += delta; c.l0 *= c.alpha; c.l1 *= c.alpha;
        const float nm = -c.m;
#pragma unroll
        for (int r = 0; r < 16; ++r) {
          S[0][r] -= delta; S[1][r] -= delta;
          asm volatile("v_mov_b32 %0, %1" : "+v"(c.NEGM[r]) : "v"(nm));
        }
        pend = true;
      }
    }
  } else {
    constexpr int u = G - 5;
    constexpr int e0 = (u * 16) / 11, e1 = ((u + 1) * 16) / 11;
    static_for<e0, e1>([&](auto EE) { constexpr int e = decltype(EE)::value; float t = __builtin_amdgcn_exp2f(S[0][e]); PIN(t); S[0][e] = t; });
    constexpr int a0 = u == 0 ? 0 : ((u - 1) * 16) / 11, a1 = u == 0 ? 0 : (G == 15 ? 16 : e0);
    static_for<a0, a1>([&](auto EE) { constexpr int e = decltype(EE)::value; if constexpr (e & 1) { c.l1 += S[0][e]; PIN(c.l1); } else { c.l0 += S[0][e]; PIN(c.l0); } });
  }
}

template <int X, bool QK, bool PV, bool FIRST, bool DK, bool DV, bool NEXTK, bool NDK, bool NDV>
DI void a_iter(ACtx& c, int it) {
  bool pend = false;
  static_for<0, 32>([&](auto SS) {
    constexpr int s = decltype(SS)::value;
    if constexpr (s == 29) {
      asm volatile("s_waitcnt vmcnt(0) lgkmcnt(0)\n\ts_barrier" ::: "memory");
    }
    if constexpr (A_ABL == 1) { } else
    if constexpr (s < 16) {
      if constexpr (QK) {
        constexpr int kb = s >> 3, step = s & 7;
        if constexpr (step == 0) {
          if constexpr (FIRST) c.S[X][kb] = mfma32(c.F[s & 3], c.Q[0], f32x16{});
          else                 c.S[X][kb] = mfma32(c.F[s & 3], c.Q[0], c.NEGM);
        } else c.S[X][kb] = mfma32(c.F[s & 3], c.Q[step], c.S[X][kb]);
      }
    } else {
      if constexpr (PV) { constexpr int u = s - 16, ks = u >> 2, db = u & 3; c.O[db] = mfma32(c.F[s & 3], c.P[ks], c.O[db]); }
    }
    constexpr int t = s + 3;
    if constexpr (A_ABL == 1) { } else
    if constexpr (t < 16) { if constexpr (QK) a_read<t, X, X ^ 1>(c); }
    else if constexpr (t < 32) { if constexpr (PV) a_read<t, X, X ^ 1>(c); }
    else { if constexpr (NEXTK) a_read<t - 32, X ^ 1, X>(c); }
    if constexpr (A_ABL != 2) {
      if constexpr (s < 5) a_dma<X, s + 3, DK, DV>(c, it);
      if constexpr (s >= 29) a_dma<X ^ 1, s - 29, NDK, NDV>(c, it + 1);
    }
    if constexpr (A_ABL == 1) { } else
    if constexpr (s < 16) { if constexpr (!FIRST) a_part2<X ^ 1, s>(c); }
    else { if constexpr (QK) a_part1<X, s - 16, FIRST>(c, pend); }
    SBAR();
  });
  if constexpr (QK && !FIRST && PV) {
    if (__builtin_expect(pend, 0)) {
#pragma unroll
      for (int d = 0; d < 4; ++d)
#pragma unroll
        for (int r = 0; r < 16; ++r) c.O[d][r] *= c.alpha;
    }
  }
}

__global__ __launch_bounds__(512, 2) void attn_kernel(const __hip_bfloat16* __restrict__ Qs, const __hip_bfloat16* __restrict__ kc,
                                                      const __hip_bfloat16* __restrict__ vc, __hip_bfloat16* __restrict__ Og) {
  __shared__ __attribute__((aligned(1024))) char lds[A_LDS];
  const unsigned ldsbase = (unsigned)(uintptr_t)LDSP(char, lds);
  const int bid = blockIdx.x, xcd = bid & 7, j = bid >> 3;
  const int pair = j >> 1, half = j & 1, hkv = xcd & 3, b = (xcd >> 2) * 16 + pair;
  const int tid = threadIdx.x, lane = tid & 63;
  const int wid = __builtin_amdgcn_readfirstlane(tid >> 6);
  const int rb = wid & 3, kh = wid >> 2;
  const int head = hkv * 8 + half * 4 + rb;
  STAMP(2, 0);
  ACtx c;
  const size_t kvoff = (size_t)(b * HKV_ + hkv) * SK_ * DH_;
  c.srdK = __builtin_amdgcn_make_buffer_rsrc((void*)(kc + kvoff), 0, SK_ * DH_ * 2, 0x00020000);
  c.srdV = __builtin_amdgcn_make_buffer_rsrc((void*)(vc + kvoff), 0, SK_ * DH_ * 2, 0x00020000);
  c.srdR[0] = half == 0 ? c.srdK : c.srdV; c.srdR[1] = half == 0 ? c.srdV : c.srdK;
  {
    const int jr = lane >> 4, sl = lane & 15;
#pragma unroll
    for (int k = 0; k < 4; ++k) c.voffK[k] = (unsigned)(jr * 256 + ((sl ^ ((4 * k + jr) & 15)) << 4));
    c.voffV = (unsigned)(jr * 256 + ((sl ^ (jr << 2)) << 4));
#pragma unroll
    for (int s = 0; s < 2; ++s) {
      c.m0K[s] = ldsbase + A_KOFF + s * A_STG + kh * 16384 + rb * 4096;
      c.m0V[s] = ldsbase + A_VOFF + s * A_STG + kh * 16384 + rb * 4096;
    }
    c.soffbase = (unsigned)(kh * 16384 + rb * 4096);
    c.kfirst = (half == 0);
#pragma unroll
    for (int r = 0; r < 2; ++r) {
      const bool isK = (r == 0) == c.kfirst;
#pragma unroll
      for (int k = 0; k < 4; ++k) c.voffR[r][k] = isK ? c.voffK[k] : c.voffV;
#pragma unroll
      for (int x = 0; x < 2; ++x) c.m0R[r][x] = isK ? c.m0K[x ^ 1] : c.m0V[x];
      c.tadd[r] = isK ? 1u : 0u;
    }
    const int kr = lane & 31, hi = lane >> 5;
#pragma unroll
    for (int st = 0; st < 8; ++st) c.kaddr[st] = ldsbase + A_KOFF + (unsigned)(kh * 16384 + kr * 256 + (((2 * st + hi) ^ (kr & 15)) << 4));
    const int g = lane >> 4, hh = g >> 1, ch = g & 1, q = (lane >> 2) & 3, p = lane & 3;
#pragma unroll
    for (int db = 0; db < 4; ++db) c.vaddr[db] = ldsbase + A_VOFF + (unsigned)(kh * 16384 + (4 * hh + q) * 256 + ((db ^ q) << 6) + ch * 32 + p * 8);
  }
#pragma unroll
  for (int i = 0; i < 8; ++i) PIN(c.kaddr[i]);
#pragma unroll
  for (int i = 0; i < 4; ++i) { PIN(c.vaddr[i]); PIN(c.voffK[i]); PIN(c.voffR[0][i]); PIN(c.voffR[1][i]); }
  PIN(c.voffV);
#pragma unroll
  for (int k = 0; k < 4; ++k) {
    if (k == 0) dma16<0, KV_POL>(c.m0K[0], c.voffK[0], c.srdK, c.soffbase);
    if (k == 1) dma16<1024, KV_POL>(c.m0K[0], c.voffK[1], c.srdK, c.soffbase);
    if (k == 2) dma16<2048, KV_POL>(c.m0K[0], c.voffK[2], c.srdK, c.soffbase);
    if (k == 3) dma16<3072, KV_POL>(c.m0K[0], c.voffK[3], c.srdK, c.soffbase);
  }
  dma16<0, KV_POL>(c.m0K[1], c.voffK[0], c.srdK, c.soffbase + 32768u);
  dma16<1024, KV_POL>(c.m0K[1], c.voffK[1], c.srdK, c.soffbase + 32768u);
  dma16<2048, KV_POL>(c.m0K[1], c.voffK[2], c.srdK, c.soffbase + 32768u);
  dma16<3072, KV_POL>(c.m0K[1], c.voffK[3], c.srdK, c.soffbase + 32768u);
  dma16<0, KV_POL>(c.m0V[0], c.voffV, c.srdV, c.soffbase);
  dma16<1024, KV_POL>(c.m0V[0], c.voffV, c.srdV, c.soffbase);
  dma16<2048, KV_POL>(c.m0V[0], c.voffV, c.srdV, c.soffbase);
  dma16<3072, KV_POL>(c.m0V[0], c.voffV, c.srdV, c.soffbase);
  {
    const bf16x8* qp = reinterpret_cast<const bf16x8*>(Qs + (size_t)(b * SQ_ + (lane & 31)) * QCOLS + head * DH_ + (lane >> 5) * 8);
#pragma unroll
    for (int st = 0; st < 8; ++st) c.Q[st] = qp[2 * st];
  }
#pragma unroll
  for (int d = 0; d < 4; ++d) c.O[d] = f32x16{};
  c.l0 = 0.f; c.l1 = 0.f; c.m = 0.f; c.alpha = 1.f;
  asm volatile("s_waitcnt vmcnt(0)\n\ts_barrier" ::: "memory");
  STAMP(2, 1);
  a_read<0, 0, 1>(c); a_read<1, 0, 1>(c); a_read<2, 0, 1>(c);
  a_iter<0, true, false, true, false, false, true, true, true>(c, 0);
  for (int it = 1; it + 1 < A_NIT - 3; it += 2) {
    a_iter<1, true, true, false, true, true, true, true, true>(c, it);
    a_iter<0, true, true, false, true, true, true, true, true>(c, it + 1);
  }
  a_iter<1, true, true, false, true, true, true, true, true>(c, A_NIT - 3);
  a_iter<0, true, true, false, true, true, true, false, true>(c, A_NIT - 2);
  a_iter<1, true, true, false, false, true, false, false, false>(c, A_NIT - 1);
  a_iter<0, false, true, false, false, false, false, false, false>(c, A_NIT);

  STAMP(2, 2);
  float l = c.l0 + c.l1;
  { auto pr = __builtin_amdgcn_permlane32_swap(__float_as_uint(l), __float_as_uint(l), false, false);
    l = __uint_as_float(pr[0]) + __uint_as_float(pr[1]); }
  asm volatile("s_waitcnt lgkmcnt(0)\n\ts_barrier" ::: "memory");
  const unsigned xbase = ldsbase + rb * 17408;
  if (kh == 1) {
#pragma unroll
    for (int d = 0; d < 4; ++d)
#pragma unroll
      for (int rq = 0; rq < 4; ++rq) {
        f32x4 v = {c.O[d][4 * rq], c.O[d][4 * rq + 1], c.O[d][4 * rq + 2], c.O[d][4 * rq + 3]};
        *LDSP(f32x4, xbase + (d * 4 + rq) * 1024 + lane * 16) = v;
      }
    f32x2 ml = {c.m, l};
    *LDSP(f32x2, xbase + 16384 + lane * 8) = ml;
  }
  asm volatile("s_waitcnt lgkmcnt(0)\n\ts_barrier" ::: "memory");
  if (kh == 0) {
    const f32x2 ml = *LDSP(const f32x2, xbase + 16384 + lane * 8);
    const float mnew = fmaxf(c.m, ml[0]);
    const float f0 = __builtin_amdgcn_exp2f(c.m - mnew), f1 = __builtin_amdgcn_exp2f(ml[0] - mnew);
    const float inv = 1.0f / (f0 * l + f1 * ml[1]);
    const float g0 = f0 * inv, g1 = f1 * inv;
    const int qrow = lane & 31, hi = lane >> 5;
#pragma unroll
    for (int d = 0; d < 4; ++d)
#pragma unroll
      for (int rq = 0; rq < 4; ++rq) {
        const f32x4 v = *LDSP(const f32x4, xbase + (d * 4 + rq) * 1024 + lane * 16);
        const float o0 = c.O[d][4 * rq] * g0 + v[0] * g1, o1 = c.O[d][4 * rq + 1] * g0 + v[1] * g1;
        const float o2 = c.O[d][4 * rq + 2] * g0 + v[2] * g1, o3 = c.O[d][4 * rq + 3] * g0 + v[3] * g1;
        u32x2 w = {cvtpk(o0, o1), cvtpk(o2, o3)};
        c.O[d][4 * rq] = __uint_as_float(w[0]); c.O[d][4 * rq + 1] = __uint_as_float(w[1]);
      }
    asm volatile("s_waitcnt lgkmcnt(0)" ::: "memory");
#pragma unroll
    for (int d = 0; d < 4; ++d)
#pragma unroll
      for (int rq = 0; rq < 4; ++rq) {
        u32x2 w = {__float_as_uint(c.O[d][4 * rq]), __float_as_uint(c.O[d][4 * rq + 1])};
        *LDSP(u32x2, xbase + qrow * 272 + (32 * d + 8 * rq + 4 * hi) * 2) = w;
      }
    asm volatile("s_waitcnt lgkmcnt(0)" ::: "memory");
    __hip_bfloat16* og = Og + (size_t)(b * SQ_) * QCOLS + head * DH_;
#pragma unroll
    for (int it = 0; it < 8; ++it) {
      const int row = it * 4 + (lane >> 4), ck = lane & 15;
      u32x4 v = *LDSP(const u32x4, xbase + row * 272 + ck * 16);
      *reinterpret_cast<u32x4*>(og + (size_t)row * QCOLS + ck * 8) = v;
    }
  }
  STAMP(2, 3);
}

extern "C" void kernel_launch(void* const* d_in, const int* in_sizes, int n_in, void* d_out, int out_size, void* d_ws, size_t ws_size,
                              hipStream_t stream) {
  const __hip_bfloat16* x = (const __hip_bfloat16*)d_in[0];
  const __hip_bfloat16* kc = (const __hip_bfloat16*)d_in[1];
  const __hip_bfloat16* vc = (const __hip_bfloat16*)d_in[2];
  const __hip_bfloat16* wq = (const __hip_bfloat16*)d_in[3];
  const __hip_bfloat16* wo = (const __hip_bfloat16*)d_in[4];
  char* ws = (char*)d_ws;
  __hip_bfloat16* Qs = (__hip_bfloat16*)ws;
  __hip_bfloat16* Ob = (__hip_bfloat16*)(ws + (size_t)8 * 1024 * 1024);
  float* part = (float*)(ws + (size_t)16 * 1024 * 1024);
  qproj_kernel<<<256, 512, 0, stream>>>(x, wq, Qs);
  attn_kernel<<<256, 512, 0, stream>>>(Qs, kc, vc, Ob);
  oproj_kernel<<<256, 512, 0, stream>>>(Ob, wo, part);
  oreduce_kernel<<<MROWS * DM_ / 8 / 256, 256, 0, stream>>>(part, (__hip_bfloat16*)d_out);
}
```

```cpp
#include <hip/hip_runtime.h>
#include <hip/hip_bf16.h>
#include <stdint.h>
#define STAMP(K, I)
#define A_ABL 0
#define G_ABL 0

typedef __attribute__((ext_vector_type(8))) __bf16 bf16x8;
typedef __attribute__((ext_vector_type(4))) __bf16 bf16x4;
typedef __attribute__((ext_vector_type(2))) __bf16 bf16x2;
typedef __attribute__((ext_vector_type(16))) float f32x16;
typedef __attribute__((ext_vector_type(4))) float f32x4;
typedef __attribute__((ext_vector_type(2))) float f32x2;
typedef __attribute__((ext_vector_type(4))) unsigned u32x4;
typedef __attribute__((ext_vector_type(2))) unsigned u32x2;

#define DI __device__ __forceinline__
#define LDSP(T, a) ((__attribute__((address_space(3))) T*)(uintptr_t)(a))
#define SBAR() __builtin_amdgcn_sched_barrier(0)

constexpr int NB_ = 32, SQ_ = 32, DM_ = 1024, HQ_ = 32, HKV_ = 4, SK_ = 2048, DH_ = 128;
constexpr int MROWS = NB_ * SQ_;
constexpr int QCOLS = HQ_ * DH_;
constexpr float QSCALE = 0.08838834764831845f * 1.4426950408889634f;

DI unsigned cvtpk(float lo, float hi) {
  f32x2 v = {lo, hi}; bf16x2 b = __builtin_convertvector(v, bf16x2); return __builtin_bit_cast(unsigned, b);
}
DI float max3f(float a, float b, float c) { float r; asm("v_max3_f32 %0, %1, %2, %3" : "=v"(r) : "v"(a), "v"(b), "v"(c)); return r; }
DI float max2f(float a, float b) { float r; asm("v_max_f32_e32 %0, %1, %2" : "=v"(r) : "v"(a), "v"(b)); return r; }
#define PIN(x) asm volatile("" : "+v"(x))
DI f32x16 mfma32(bf16x8 a, bf16x8 b, f32x16 c) { return __builtin_amdgcn_mfma_f32_32x32x16_bf16(a, b, c, 0, 0, 0); }

template <int IMM, int POL = 0> DI void dma16(unsigned m0v, unsigned voff, __amdgpu_buffer_rsrc_t srd, unsigned soff) {
  if constexpr (POL == 0)
    asm volatile("s_mov_b32 m0, %0\n\ts_nop 0\n\tbuffer_load_dwordx4 %1, %2, %3 offen offset:%4 lds"
                 :: "s"(m0v), "v"(voff), "s"(srd), "s"(soff), "i"(IMM) : "memory");
  else if constexpr (POL == 1)
    asm volatile("s_mov_b32 m0, %0\n\ts_nop 0\n\tbuffer_load_dwordx4 %1, %2, %3 offen offset:%4 nt lds"
                 :: "s"(m0v), "v"(voff), "s"(srd), "s"(soff), "i"(IMM) : "memory");
  else if constexpr (POL == 2)
    asm volatile("s_mov_b32 m0, %0\n\ts_nop 0\n\tbuffer_load_dwordx4 %1, %2, %3 offen offset:%4 sc1 lds"
                 :: "s"(m0v), "v"(voff), "s"(srd), "s"(soff), "i"(IMM) : "memory");
  else if constexpr (POL == 3)
    asm volatile("s_mov_b32 m0, %0\n\ts_nop 0\n\tbuffer_load_dwordx4 %1, %2, %3 offen offset:%4 sc0 sc1 lds"
                 :: "s"(m0v), "v"(voff), "s"(srd), "s"(soff), "i"(IMM) : "memory");
  else
    asm volatile("s_mov_b32 m0, %0\n\ts_nop 0\n\tbuffer_load_dwordx4 %1, %2, %3 offen offset:%4 sc1 nt lds"
                 :: "s"(m0v), "v"(voff), "s"(srd), "s"(soff), "i"(IMM) : "memory");
}
#define KV_POL 0
#define KV_POL2 KV_POL
#define GA_POL 0
#define GB_POL 0
DI bf16x8 lds_b128(unsigned a) { return *LDSP(const bf16x8, a); }
DI bf16x4 lds_tr(unsigned a) { return __builtin_amdgcn_ds_read_tr16_b64_v4bf16(LDSP(bf16x4, a)); }
DI bf16x8 cat8(bf16x4 a, bf16x4 b) { return __builtin_shufflevector(a, b, 0, 1, 2, 3, 4, 5, 6, 7); }

template <int I> struct IC { static constexpr int value = I; };
template <int B, int E, class F> DI void static_for(F&& f) {
  if constexpr (B < E) { f(IC<B>{}); static_for<B + 1, E>(static_cast<F&&>(f)); }
}

constexpr int G_NS = 4;
constexpr int G_STAGE = 32768;

template <int LDA, int LDB, int KLEN>
DI void gemm_tile(const __hip_bfloat16* __restrict__ Ag, unsigned Abytes, const __hip_bfloat16* __restrict__ Bg, unsigned Bbytes,
                  unsigned ldsbase, int rot, f32x16 (&acc)[2][2]) {
  constexpr int NT = KLEN / 64;
  static_assert((NT & (NT - 1)) == 0 && NT >= 4, "power-of-two tile count");
  const int tid = threadIdx.x, lane = tid & 63;
  const int wid = __builtin_amdgcn_readfirstlane(tid >> 6);
  if (wid >= 4) {
    const int lw = wid - 4;
    auto srdA = __builtin_amdgcn_make_buffer_rsrc((void*)Ag, 0, Abytes, 0x00020000);
    auto srdB = __builtin_amdgcn_make_buffer_rsrc((void*)Bg, 0, Bbytes, 0x00020000);
    const unsigned voffA0 = (unsigned)((lane >> 3) * LDA * 2 + ((((lane & 7) ^ ((lane >> 4) & 7))) << 4));
    const unsigned voffA1 = (unsigned)((lane >> 3) * LDA * 2 + ((((lane & 7) ^ ((4 + (lane >> 4)) & 7))) << 4));
    const unsigned voffB = (unsigned)((lane >> 4) * LDB * 2 + (((lane & 15) ^ (((lane >> 4) & 3) << 2)) << 4));
    auto issue = [&](int kt) {
      const int kr = (kt + rot) & (NT - 1);
      const unsigned mA = ldsbase + (unsigned)(kt & (G_NS - 1)) * G_STAGE + lw * 4096, mB = mA + 16384;
      const unsigned sA = (unsigned)(kr * 128 + lw * 4 * 16 * LDA), sB = (unsigned)((kr * 64 + lw * 16) * LDB * 2);
      dma16<0, GA_POL>(mA, voffA0, srdA, sA);
      dma16<0, GB_POL>(mB, voffB, srdB, sB);
      dma16<0, GA_POL>(mA + 1024, voffA1, srdA, sA + 16 * LDA);
      dma16<0, GB_POL>(mB + 1024, voffB, srdB, sB + 4 * LDB * 2);
      dma16<0, GA_POL>(mA + 2048, voffA0, srdA, sA + 32 * LDA);
      dma16<0, GB_POL>(mB + 2048, voffB, srdB, sB + 8 * LDB * 2);
      dma16<0, GA_POL>(mA + 3072, voffA1, srdA, sA + 48 * LDA);
      dma16<0, GB_POL>(mB + 3072, voffB, srdB, sB + 12 * LDB * 2);
    };
    issue(0); issue(1); issue(2);
    asm volatile("s_waitcnt vmcnt(16)\n\ts_barrier" ::: "memory");
    for (int kt = 0; kt < NT; ++kt) {
      if (kt + 2 < NT) asm volatile("s_waitcnt vmcnt(8)\n\ts_barrier" ::: "memory");
      else             asm volatile("s_waitcnt vmcnt(0)\n\ts_barrier" ::: "memory");
      if (kt + 3 < NT) issue(kt + 3);
    }
    return;
  }
  const int wm = wid >> 1, wn = wid & 1;
  const int m = lane & 31, h = lane >> 5;
  unsigned aaddr[4];
#pragma unroll
  for (int ks = 0; ks < 4; ++ks) aaddr[ks] = ldsbase + (unsigned)((wm * 64 + m) * 128 + (((2 * ks + h) ^ ((m >> 1) & 7)) << 4));
  const int g = lane >> 4, hh = g >> 1, ch = g & 1, q = (lane >> 2) & 3, p = lane & 3;
  unsigned baddr[2];
#pragma unroll
  for (int nb = 0; nb < 2; ++nb) baddr[nb] = ldsbase + 16384 + (unsigned)((8 * hh + q) * 256 + (((wn * 2 + nb) ^ q) << 6) + ch * 32 + p * 8);
#pragma unroll
  for (int i = 0; i < 2; ++i)
#pragma unroll
    for (int j = 0; j < 2; ++j) acc[i][j] = f32x16{};
  auto rdA = [&](unsigned stg, int ks, int mb) { return lds_b128(aaddr[ks] + stg + mb * 4096); };
  auto rdB = [&](unsigned stg, int ks, int nb) {
    return cat8(lds_tr(baddr[nb] + stg + (16 * ks) * 256), lds_tr(baddr[nb] + stg + (16 * ks + 4) * 256)); };
  asm volatile("s_barrier" ::: "memory");
  STAMP((LDA == DM_ ? 0 : 1), 1);
  bf16x8 af[2][2], bfr[2][2];
  af[0][0] = rdA(0, 0, 0); af[0][1] = rdA(0, 0, 1); bfr[0][0] = rdB(0, 0, 0); bfr[0][1] = rdB(0, 0, 1);
  unsigned stg = 0;
  for (int kt = 0; kt < NT; ++kt) {
    asm volatile("s_barrier" ::: "memory");
    const unsigned nstg = (stg + G_STAGE) & (G_NS * G_STAGE - 1);
    static_for<0, 16>([&](auto SS) {
      constexpr int s = decltype(SS)::value, ks = s >> 2, sub = s & 3, cur = ks & 1, nxt = cur ^ 1;
      constexpr int nb = sub >> 1, mb = sub & 1;
      acc[nb][mb] = mfma32(bfr[cur][nb], af[cur][mb], acc[nb][mb]);
      if constexpr (sub < 2) {
        if constexpr (ks < 3) { af[nxt][sub] = rdA(stg, ks + 1, sub); bfr[nxt][sub] = rdB(stg, ks + 1, sub); }
        else                  { af[nxt][sub] = rdA(nstg, 0, sub);     bfr[nxt][sub] = rdB(nstg, 0, sub); }
      }
      SBAR();
    });
    stg = nstg;
  }
}

__global__ __launch_bounds__(512, 2) void qproj_kernel(const __hip_bfloat16* __restrict__ x, const __hip_bfloat16* __restrict__ wq,
                                                       __hip_bfloat16* __restrict__ Qs) {
  __shared__ __attribute__((aligned(1024))) char lds[G_NS * G_STAGE];
  const unsigned ldsbase = (unsigned)(uintptr_t)LDSP(char, lds);
  const int bid = blockIdx.x, xcd = bid & 7, j = bid >> 3;
  const int nt = xcd * 4 + (j & 3), mt = j >> 2;
  STAMP(0, 0);
  f32x16 acc[2][2];
  gemm_tile<DM_, QCOLS, DM_>(x + (size_t)mt * 128 * DM_, (unsigned)((MROWS - mt * 128) * DM_ * 2),
                             wq + nt * 128, (unsigned)((DM_ * QCOLS - nt * 128) * 2), ldsbase, (2 * mt + 5 * (j & 3)) & 15, acc);
  STAMP(0, 2);
  const int lane = threadIdx.x & 63, wid = threadIdx.x >> 6, wm = wid >> 1, wn = wid & 1, m = lane & 31, h = lane >> 5;
  asm volatile("s_waitcnt lgkmcnt(0)\n\ts_barrier" ::: "memory");
  if (wid < 4) {
#pragma unroll
    for (int nb = 0; nb < 2; ++nb)
#pragma unroll
      for (int mb = 0; mb < 2; ++mb)
#pragma unroll
        for (int rq = 0; rq < 4; ++rq) {
          u32x2 w = {cvtpk(acc[nb][mb][4 * rq] * QSCALE, acc[nb][mb][4 * rq + 1] * QSCALE), cvtpk(acc[nb][mb][4 * rq + 2] * QSCALE, acc[nb][mb][4 * rq + 3] * QSCALE)};
          *LDSP(u32x2, ldsbase + (wm * 64 + mb * 32 + m) * 272 + (wn * 64 + nb * 32 + 8 * rq + 4 * h) * 2) = w;
        }
  }
  asm volatile("s_waitcnt lgkmcnt(0)\n\ts_barrier" ::: "memory");
  __hip_bfloat16* Cg = Qs + (size_t)mt * 128 * QCOLS + nt * 128;
#pragma unroll
  for (int it = 0; it < 4; ++it) {
    const int row = it * 32 + (threadIdx.x >> 4), ck = threadIdx.x & 15;
    u32x4 v = *LDSP(const u32x4, ldsbase + row * 272 + ck * 16);
    *reinterpret_cast<u32x4*>(Cg + (size_t)row * QCOLS + ck * 8) = v;
  }
  STAMP(0, 3);
}

__global__ __launch_bounds__(512, 2) void oproj_kernel(const __hip_bfloat16* __restrict__ O, const __hip_bfloat16* __restrict__ wo,
                                                       float* __restrict__ part) {
  __shared__ __attribute__((aligned(1024))) char lds[G_NS * G_STAGE];
  const unsigned ldsbase = (unsigned)(uintptr_t)LDSP(char, lds);
  const int bid = blockIdx.x, xcd = bid & 7, j = bid >> 3;
  const int ksp = xcd >> 1, mt = (xcd & 1) * 4 + (j & 3), nt = j >> 2;
  STAMP(1, 0);
  f32x16 acc[2][2];
  gemm_tile<QCOLS, DM_, 1024>(O + (size_t)mt * 128 * QCOLS + ksp * 1024, (unsigned)(((MROWS - mt * 128) * QCOLS - ksp * 1024) * 2),
                              wo + (size_t)ksp * 1024 * DM_ + nt * 128, (unsigned)(((QCOLS - ksp * 1024) * DM_ - nt * 128) * 2), ldsbase,
                              (2 * nt + 5 * (j & 3)) & 15, acc);
  STAMP(1, 2);
  const int lane = threadIdx.x & 63, wid = threadIdx.x >> 6, wm = wid >> 1, wn = wid & 1, m = lane & 31, h = lane >> 5;
  if (wid < 4) {
    float* P = part + (size_t)ksp * MROWS * DM_ + (size_t)(mt * 128) * DM_ + nt * 128;
#pragma unroll
    for (int nb = 0; nb < 2; ++nb)
#pragma unroll
      for (int mb = 0; mb < 2; ++mb)
#pragma unroll
        for (int rq = 0; rq < 4; ++rq) {
          f32x4 v = {acc[nb][mb][4 * rq], acc[nb][mb][4 * rq + 1], acc[nb][mb][4 * rq + 2], acc[nb][mb][4 * rq + 3]};
          *reinterpret_cast<f32x4*>(P + (size_t)(wm * 64 + mb * 32 + m) * DM_ + wn * 64 + nb * 32 + 8 * rq + 4 * h) = v;
        }
  }
  STAMP(1, 3);
}

__global__ __launch_bounds__(256) void oreduce_kernel(const float* __restrict__ part, __hip_bfloat16* __restrict__ out) {
  const size_t i = ((size_t)blockIdx.x * 256 + threadIdx.x) * 8;
  f32x4 a0 = {}, a1 = {};
#pragma unroll
  for (int s = 0; s < 4; ++s) {
    a0 += *reinterpret_cast<const f32x4*>(part + (size_t)s * MROWS * DM_ + i);
    a1 += *reinterpret_cast<const f32x4*>(part + (size_t)s * MROWS * DM_ + i + 4);
  }
  u32x4 w = {cvtpk(a0[0], a0[1]), cvtpk(a0[2], a0[3]), cvtpk(a1[0], a1[1]), cvtpk(a1[2], a1[3])};
  *reinterpret_cast<u32x4*>(out + i) = w;
}

constexpr int A_KOFF = 0, A_VOFF = 65536, A_STG = 32768, A_LDS = 131072;
constexpr float A_THR = 8.0f;
constexpr int A_NIT = SK_ / 128;

struct ACtx {
  f32x16 O[4];
  bf16x8 Q[8];
  f32x16 S[2][2];
  bf16x8 P[4];
  f32x16 NEGM;
  bf16x8 F[4];
  float m, l0, l1;
  float mx0, mx1, alpha;
  unsigned kaddr[8], vaddr[4];
  unsigned voffK[4], voffV, m0K[2], m0V[2], soffbase;
  unsigned voffR[2][4], m0R[2][2], tadd[2];
  __amdgpu_buffer_rsrc_t srdR[2];
  bool kfirst;
  __amdgpu_buffer_rsrc_t srdK, srdV;
};

template <int T, int KS, int VS> DI void a_read(ACtx& c) {
  if constexpr (T < 16) {
    constexpr int kb = T >> 3, step = T & 7;
    c.F[T & 3] = lds_b128(c.kaddr[step] + KS * A_STG + kb * 8192);
  } else {
    constexpr int u = T - 16, ks = u >> 2, db = u & 3;
    c.F[T & 3] = cat8(lds_tr(c.vaddr[db] + VS * A_STG + ks * 4096), lds_tr(c.vaddr[db] + VS * A_STG + ks * 4096 + 2048));
  }
}

template <int X, int P, bool DK, bool DV> DI void a_dma(ACtx& c, int it) {
  constexpr int R = P >> 2, D = P & 3;
  if constexpr (DK && DV) {
    dma16<D * 1024, (R == 0 ? KV_POL : KV_POL2)>(c.m0R[R][X], c.voffR[R][D], c.srdR[R], c.soffbase + ((unsigned)it + c.tadd[R]) * 32768u);
  } else if constexpr (DK || DV) {
    if ((R == 0) == (c.kfirst == DK))
      dma16<D * 1024, (R == 0 ? KV_POL : KV_POL2)>(c.m0R[R][X], c.voffR[R][D], c.srdR[R], c.soffbase + ((unsigned)it + c.tadd[R]) * 32768u);
  }
}

template <int XP, int G> DI void a_part2(ACtx& c) {
  if constexpr (G < 8) {
    float e0 = __builtin_amdgcn_exp2f(c.S[XP][1][2 * G]);     PIN(e0); c.S[XP][1][2 * G] = e0;
    float e1 = __builtin_amdgcn_exp2f(c.S[XP][1][2 * G + 1]); PIN(e1); c.S[XP][1][2 * G + 1] = e1;
  } else {
    constexpr int u = G - 8;
    c.l0 += c.S[XP][1][2 * u]; PIN(c.l0);
    c.l1 += c.S[XP][1][2 * u + 1]; PIN(c.l1);
    static_for<0, 2>([&](auto EE) {
      constexpr int Dw = 2 * u + decltype(EE)::value, ks = Dw >> 2, pd = Dw & 3;
      u32x4 w = __builtin_bit_cast(u32x4, c.P[ks]);
      unsigned pk = cvtpk(c.S[XP][ks >> 1][8 * (ks & 1) + 2 * pd], c.S[XP][ks >> 1][8 * (ks & 1) + 2 * pd + 1]); PIN(pk);
      w[pd] = pk;
      c.P[ks] = __builtin_bit_cast(bf16x8, w);
    });
  }
}

template <int X, int G, bool FIRST> DI void a_part1(ACtx& c, bool& pend) {
  f32x16 (&S)[2] = c.S[X];
  if constexpr (G < 4) {
    constexpr int kb = G >> 1, r = 8 * (G & 1);
    if constexpr (G == 0)
      asm volatile("v_max3_f32 %0, %2, %3, %4\n\tv_max3_f32 %1, %5, %6, %7\n\tv_max3_f32 %0, %0, %8, %9"
                   : "=&v"(c.mx0), "=&v"(c.mx1) : "v"(S[0][0]), "v"(S[0][1]), "v"(S[0][2]), "v"(S[0][3]), "v"(S[0][4]), "v"(S[0][5]), "v"(S[0][6]), "v"(S[0][7]));
    else
      asm volatile("v_max3_f32 %0, %0, %2, %3\n\tv_max3_f32 %1, %1, %4, %5\n\tv_max3_f32 %0, %0, %6, %7\n\tv_max3_f32 %1, %1, %8, %9"
                   : "+v"(c.mx0), "+v"(c.mx1) : "v"(S[kb][r]), "v"(S[kb][r + 1]), "v"(S[kb][r + 2]), "v"(S[kb][r + 3]), "v"(S[kb][r + 4]), "v"(S[kb][r + 5]), "v"(S[kb][r + 6]), "v"(S[kb][r + 7]));
  } else if constexpr (G == 4) {
    float mm = max2f(c.mx0, c.mx1);
    auto pr = __builtin_amdgcn_permlane32_swap(__float_as_uint(mm), __float_as_uint(mm), false, false);
    const float rmax = max2f(__uint_as_float(pr[0]), __uint_as_float(pr[1]));
    if constexpr (FIRST) {
      c.m = rmax;
#pragma unroll
      for (int r = 0; r < 16; ++r) { S[0][r] -= rmax; S[1][r] -= rmax; c.NEGM[r] = -rmax; }
    } else {
      if (__builtin_expect(__any(rmax > A_THR), 0)) {
        const float delta = fmaxf(rmax, 0.f);
        c.alpha = __builtin_amdgcn_exp2f(-delta);
        c.m += delta; c.l0 *= c.alpha; c.l1 *= c.alpha;
        const float nm = -c.m;
#pragma unroll
        for (int r = 0; r < 16; ++r) {
          S[0][r] -= delta; S[1][r] -= delta;
          asm volatile("v_mov_b32 %0, %1" : "+v"(c.NEGM[r]) : "v"(nm));
        }
        pend = true;
      }
    }
  } else {
    constexpr int u = G - 5;
    constexpr int e0 = (u * 16) / 11, e1 = ((u + 1) * 16) / 11;
    static_for<e0, e1>([&](auto EE) { constexpr int e = decltype(EE)::value; float t = __builtin_amdgcn_exp2f(S[0][e]); PIN(t); S[0][e] = t; });
    constexpr int a0 = u == 0 ? 0 : ((u - 1) * 16) / 11, a1 = u == 0 ? 0 : (G == 15 ? 16 : e0);
    static_for<a0, a1>([&](auto EE) { constexpr int e = decltype(EE)::value; if constexpr (e & 1) { c.l1 += S[0][e]; PIN(c.l1); } else { c.l0 += S[0][e]; PIN(c.l0); } });
  }
}

template <int X, bool QK, bool PV, bool FIRST, bool DK, bool DV, bool NEXTK, bool NDK, bool NDV>
DI void a_iter(ACtx& c, int it) {
  bool pend = false;
  static_for<0, 32>([&](auto SS) {
    constexpr int s = decltype(SS)::value;
    if constexpr (s == 29) {
      asm volatile("s_waitcnt vmcnt(0) lgkmcnt(0)\n\ts_barrier" ::: "memory");
    }
    if constexpr (A_ABL == 1) { } else
    if constexpr (s < 16) {
      if constexpr (QK) {
        constexpr int kb = s >> 3, step = s & 7;
        if constexpr (step == 0) {
          if constexpr (FIRST) c.S[X][kb] = mfma32(c.F[s & 3], c.Q[0], f32x16{});
          else                 c.S[X][kb] = mfma32(c.F[s & 3], c.Q[0], c.NEGM);
        } else c.S[X][kb] = mfma32(c.F[s & 3], c.Q[step], c.S[X][kb]);
      }
    } else {
      if constexpr (PV) { constexpr int u = s - 16, ks = u >> 2, db = u & 3; c.O[db] = mfma32(c.F[s & 3], c.P[ks], c.O[db]); }
    }
    constexpr int t = s + 3;
    if constexpr (A_ABL == 1) { } else
    if constexpr (t < 16) { if constexpr (QK) a_read<t, X, X ^ 1>(c); }
    else if constexpr (t < 32) { if constexpr (PV) a_read<t, X, X ^ 1>(c); }
    else { if constexpr (NEXTK) a_read<t - 32, X ^ 1, X>(c); }
    if constexpr (A_ABL != 2) {
      if constexpr (s < 5) a_dma<X, s + 3, DK, DV>(c, it);
      if constexpr (s >= 29) a_dma<X ^ 1, s - 29, NDK, NDV>(c, it + 1);
    }
    if constexpr (A_ABL == 1) { } else
    if constexpr (s < 16) { if constexpr (!FIRST) a_part2<X ^ 1, s>(c); }
    else { if constexpr (QK) a_part1<X, s - 16, FIRST>(c, pend); }
    SBAR();
  });
  if constexpr (QK && !FIRST && PV) {
    if (__builtin_expect(pend, 0)) {
#pragma unroll
      for (int d = 0; d < 4; ++d)
#pragma unroll
        for (int r = 0; r < 16; ++r) c.O[d][r] *= c.alpha;
    }
  }
}

__global__ __launch_bounds__(512, 2) void attn_kernel(const __hip_bfloat16* __restrict__ Qs, const __hip_bfloat16* __restrict__ kc,
                                                      const __hip_bfloat16* __restrict__ vc, __hip_bfloat16* __restrict__ Og) {
  __shared__ __attribute__((aligned(1024))) char lds[A_LDS];
  const unsigned ldsbase = (unsigned)(uintptr_t)LDSP(char, lds);
  const int bid = blockIdx.x, xcd = bid & 7, j = bid >> 3;
  const int pair = j >> 1, half = j & 1, hkv = xcd & 3, b = (xcd >> 2) * 16 + pair;
  const int tid = threadIdx.x, lane = tid & 63;
  const int wid = __builtin_amdgcn_readfirstlane(tid >> 6);
  const int rb = wid & 3, kh = wid >> 2;
  const int head = hkv * 8 + half * 4 + rb;
  STAMP(2, 0);
  ACtx c;
  const size_t kvoff = (size_t)(b * HKV_ + hkv) * SK_ * DH_;
  c.srdK = __builtin_amdgcn_make_buffer_rsrc((void*)(kc + kvoff), 0, SK_ * DH_ * 2, 0x00020000);
  c.srdV = __builtin_amdgcn_make_buffer_rsrc((void*)(vc + kvoff), 0, SK_ * DH_ * 2, 0x00020000);
  c.srdR[0] = half == 0 ? c.srdK : c.srdV; c.srdR[1] = half == 0 ? c.srdV : c.srdK;
  {
    const int jr = lane >> 4, sl = lane & 15;
#pragma unroll
    for (int k = 0; k < 4; ++k) c.voffK[k] = (unsigned)(jr * 256 + ((sl ^ ((4 * k + jr) & 15)) << 4));
    c.voffV = (unsigned)(jr * 256 + ((sl ^ (jr << 2)) << 4));
#pragma unroll
    for (int s = 0; s < 2; ++s) {
      c.m0K[s] = ldsbase + A_KOFF + s * A_STG + kh * 16384 + rb * 4096;
      c.m0V[s] = ldsbase + A_VOFF + s * A_STG + kh * 16384 + rb * 4096;
    }
    c.soffbase = (unsigned)(kh * 16384 + rb * 4096);
    c.kfirst = (half == 0);
#pragma unroll
    for (int r = 0; r < 2; ++r) {
      const bool isK = (r == 0) == c.kfirst;
#pragma unroll
      for (int k = 0; k < 4; ++k) c.voffR[r][k] = isK ? c.voffK[k] : c.voffV;
#pragma unroll
      for (int x = 0; x < 2; ++x) c.m0R[r][x] = isK ? c.m0K[x ^ 1] : c.m0V[x];
      c.tadd[r] = isK ? 1u : 0u;
    }
    const int kr = lane & 31, hi = lane >> 5;
#pragma unroll
    for (int st = 0; st < 8; ++st) c.kaddr[st] = ldsbase + A_KOFF + (unsigned)(kh * 16384 + kr * 256 + (((2 * st + hi) ^ (kr & 15)) << 4));
    const int g = lane >> 4, hh = g >> 1, ch = g & 1, q = (lane >> 2) & 3, p = lane & 3;
#pragma unroll
    for (int db = 0; db < 4; ++db) c.vaddr[db] = ldsbase + A_VOFF + (unsigned)(kh * 16384 + (4 * hh + q) * 256 + ((db ^ q) << 6) + ch * 32 + p * 8);
  }
#pragma unroll
  for (int i = 0; i < 8; ++i) PIN(c.kaddr[i]);
#pragma unroll
  for (int i = 0; i < 4; ++i) { PIN(c.vaddr[i]); PIN(c.voffK[i]); PIN(c.voffR[0][i]); PIN(c.voffR[1][i]); }
  PIN(c.voffV);
#pragma unroll
  for (int k = 0; k < 4; ++k) {
    if (k == 0) dma16<0, KV_POL>(c.m0K[0], c.voffK[0], c.srdK, c.soffbase);
    if (k == 1) dma16<1024, KV_POL>(c.m0K[0], c.voffK[1], c.srdK, c.soffbase);
    if (k == 2) dma16<2048, KV_POL>(c.m0K[0], c.voffK[2], c.srdK, c.soffbase);
    if (k == 3) dma16<3072, KV_POL>(c.m0K[0], c.voffK[3], c.srdK, c.soffbase);
  }
  dma16<0, KV_POL>(c.m0K[1], c.voffK[0], c.srdK, c.soffbase + 32768u);
  dma16<1024, KV_POL>(c.m0K[1], c.voffK[1], c.srdK, c.soffbase + 32768u);
  dma16<2048, KV_POL>(c.m0K[1], c.voffK[2], c.srdK, c.soffbase + 32768u);
  dma16<3072, KV_POL>(c.m0K[1], c.voffK[3], c.srdK, c.soffbase + 32768u);
  dma16<0, KV_POL>(c.m0V[0], c.voffV, c.srdV, c.soffbase);
  dma16<1024, KV_POL>(c.m0V[0], c.voffV, c.srdV, c.soffbase);
  dma16<2048, KV_POL>(c.m0V[0], c.voffV, c.srdV, c.soffbase);
  dma16<3072, KV_POL>(c.m0V[0], c.voffV, c.srdV, c.soffbase);
  {
    const bf16x8* qp = reinterpret_cast<const bf16x8*>(Qs + (size_t)(b * SQ_ + (lane & 31)) * QCOLS + head * DH_ + (lane >> 5) * 8);
#pragma unroll
    for (int st = 0; st < 8; ++st) c.Q[st] = qp[2 * st];
  }
#pragma unroll
  for (int d = 0; d < 4; ++d) c.O[d] = f32x16{};
  c.l0 = 0.f; c.l1 = 0.f; c.m = 0.f; c.alpha = 1.f;
  asm volatile("s_waitcnt vmcnt(0)\n\ts_barrier" ::: "memory");
  STAMP(2, 1);
  a_read<0, 0, 1>(c); a_read<1, 0, 1>(c); a_read<2, 0, 1>(c);
  a_iter<0, true, false, true, false, false, true, true, true>(c, 0);
  for (int it = 1; it + 1 < A_NIT - 3; it += 2) {
    a_iter<1, true, true, false, true, true, true, true, true>(c, it);
    a_iter<0, true, true, false, true, true, true, true, true>(c, it + 1);
  }
  a_iter<1, true, true, false, true, true, true, true, true>(c, A_NIT - 3);
  a_iter<0, true, true, false, true, true, true, false, true>(c, A_NIT - 2);
  a_iter<1, true, true, false, false, true, false, false, false>(c, A_NIT - 1);
  a_iter<0, false, true, false, false, false, false, false, false>(c, A_NIT);

  STAMP(2, 2);
  float l = c.l0 + c.l1;
  { auto pr = __builtin_amdgcn_permlane32_swap(__float_as_uint(l), __float_as_uint(l), false, false);
    l = __uint_as_float(pr[0]) + __uint_as_float(pr[1]); }
  asm volatile("s_waitcnt lgkmcnt(0)\n\ts_barrier" ::: "memory");
  const unsigned xbase = ldsbase + rb * 17408;
  if (kh == 1) {
#pragma unroll
    for (int d = 0; d < 4; ++d)
#pragma unroll
      for (int rq = 0; rq < 4; ++rq) {
        f32x4 v = {c.O[d][4 * rq], c.O[d][4 * rq + 1], c.O[d][4 * rq + 2], c.O[d][4 * rq + 3]};
        *LDSP(f32x4, xbase + (d * 4 + rq) * 1024 + lane * 16) = v;
      }
    f32x2 ml = {c.m, l};
    *LDSP(f32x2, xbase + 16384 + lane * 8) = ml;
  }
  asm volatile("s_waitcnt lgkmcnt(0)\n\ts_barrier" ::: "memory");
  if (kh == 0) {
    const f32x2 ml = *LDSP(const f32x2, xbase + 16384 + lane * 8);
    const float mnew = fmaxf(c.m, ml[0]);
    const float f0 = __builtin_amdgcn_exp2f(c.m - mnew), f1 = __builtin_amdgcn_exp2f(ml[0] - mnew);
    const float inv = 1.0f / (f0 * l + f1 * ml[1]);
    const float g0 = f0 * inv, g1 = f1 * inv;
    const int qrow = lane & 31, hi = lane >> 5;
#pragma unroll
    for (int d = 0; d < 4; ++d)
#pragma unroll
      for (int rq = 0; rq < 4; ++rq) {
        const f32x4 v = *LDSP(const f32x4, xbase + (d * 4 + rq) * 1024 + lane * 16);
        const float o0 = c.O[d][4 * rq] * g0 + v[0] * g1, o1 = c.O[d][4 * rq + 1] * g0 + v[1] * g1;
        const float o2 = c.O[d][4 * rq + 2] * g0 + v[2] * g1, o3 = c.O[d][4 * rq + 3] * g0 + v[3] * g1;
        u32x2 w = {cvtpk(o0, o1), cvtpk(o2, o3)};
        c.O[d][4 * rq] = __uint_as_float(w[0]); c.O[d][4 * rq + 1] = __uint_as_float(w[1]);
      }
    asm volatile("s_waitcnt lgkmcnt(0)" ::: "memory");
#pragma unroll
    for (int d = 0; d < 4; ++d)
#pragma unroll
      for (int rq = 0; rq < 4; ++rq) {
        u32x2 w = {__float_as_uint(c.O[d][4 * rq]), __float_as_uint(c.O[d][4 * rq + 1])};
        *LDSP(u32x2, xbase + qrow * 272 + (32 * d + 8 * rq + 4 * hi) * 2) = w;
      }
    asm volatile("s_waitcnt lgkmcnt(0)" ::: "memory");
    __hip_bfloat16* og = Og + (size_t)(b * SQ_) * QCOLS + head * DH_;
#pragma unroll
    for (int it = 0; it < 8; ++it) {
      const int row = it * 4 + (lane >> 4), ck = lane & 15;
      u32x4 v = *LDSP(const u32x4, xbase + row * 272 + ck * 16);
      *reinterpret_cast<u32x4*>(og + (size_t)row * QCOLS + ck * 8) = v;
    }
  }
  STAMP(2, 3);
}

extern "C" void kernel_launch(void* const* d_in, const int* in_sizes, int n_in, void* d_out, int out_size, void* d_ws, size_t ws_size,
                              hipStream_t stream) {
  const __hip_bfloat16* x = (const __hip_bfloat16*)d_in[0];
  const __hip_bfloat16* kc = (const __hip_bfloat16*)d_in[1];
  const __hip_bfloat16* vc = (const __hip_bfloat16*)d_in[2];
  const __hip_bfloat16* wq = (const __hip_bfloat16*)d_in[3];
  const __hip_bfloat16* wo = (const __hip_bfloat16*)d_in[4];
  char* ws = (char*)d_ws;
  __hip_bfloat16* Qs = (__hip_bfloat16*)ws;
  __hip_bfloat16* Ob = (__hip_bfloat16*)(ws + (size_t)8 * 1024 * 1024);
  float* part = (float*)(ws + (size_t)16 * 1024 * 1024);
  qproj_kernel<<<256, 512, 0, stream>>>(x, wq, Qs);
  attn_kernel<<<256, 512, 0, stream>>>(Qs, kc, vc, Ob);
  oproj_kernel<<<256, 512, 0, stream>>>(Ob, wo, part);
  oreduce_kernel<<<MROWS * DM_ / 8 / 256, 256, 0, stream>>>(part, (__hip_bfloat16*)d_out);
}
```

```cpp
#include <hip/hip_runtime.h>
#include <hip/hip_bf16.h>
#include <stdint.h>
#define STAMP(K, I)
#define A_ABL 0
#define G_ABL 0

typedef __attribute__((ext_vector_type(8))) __bf16 bf16x8;
typedef __attribute__((ext_vector_type(4))) __bf16 bf16x4;
typedef __attribute__((ext_vector_type(2))) __bf16 bf16x2;
typedef __attribute__((ext_vector_type(16))) float f32x16;
typedef __attribute__((ext_vector_type(4))) float f32x4;
typedef __attribute__((ext_vector_type(2))) float f32x2;
typedef __attribute__((ext_vector_type(4))) unsigned u32x4;
typedef __attribute__((ext_vector_type(2))) unsigned u32x2;

#define DI __device__ __forceinline__
#define LDSP(T, a) ((__attribute__((address_space(3))) T*)(uintptr_t)(a))
#define SBAR() __builtin_amdgcn_sched_barrier(0)

constexpr int NB_ = 32, SQ_ = 32, DM_ = 1024, HQ_ = 32, HKV_ = 4, SK_ = 2048, DH_ = 128;
constexpr int MROWS = NB_ * SQ_;
constexpr int QCOLS = HQ_ * DH_;
constexpr float QSCALE = 0.08838834764831845f * 1.4426950408889634f;

DI unsigned cvtpk(float lo, float hi) {
  f32x2 v = {lo, hi}; bf16x2 b = __builtin_convertvector(v, bf16x2); return __builtin_bit_cast(unsigned, b);
}
DI float max3f(float a, float b, float c) { float r; asm("v_max3_f32 %0, %1, %2, %3" : "=v"(r) : "v"(a), "v"(b), "v"(c)); return r; }
DI float max2f(float a, float b) { float r; asm("v_max_f32_e32 %0, %1, %2" : "=v"(r) : "v"(a), "v"(b)); return r; }
#define PIN(x) asm volatile("" : "+v"(x))
DI f32x16 mfma32(bf16x8 a, bf16x8 b, f32x16 c) { return __builtin_amdgcn_mfma_f32_32x32x16_bf16(a, b, c, 0, 0, 0); }

template <int IMM, int POL = 0> DI void dma16(unsigned m0v, unsigned voff, __amdgpu_buffer_rsrc_t srd, unsigned soff) {
  if constexpr (POL == 0)
    asm volatile("s_mov_b32 m0, %0\n\ts_nop 0\n\tbuffer_load_dwordx4 %1, %2, %3 offen offset:%4 lds"
                 :: "s"(m0v), "v"(voff), "s"(srd), "s"(soff), "i"(IMM) : "memory");
  else if constexpr (POL == 1)
    asm volatile("s_mov_b32 m0, %0\n\ts_nop 0\n\tbuffer_load_dwordx4 %1, %2, %3 offen offset:%4 nt lds"
                 :: "s"(m0v), "v"(voff), "s"(srd), "s"(soff), "i"(IMM) : "memory");
  else if constexpr (POL == 2)
    asm volatile("s_mov_b32 m0, %0\n\ts_nop 0\n\tbuffer_load_dwordx4 %1, %2, %3 offen offset:%4 sc1 lds"
                 :: "s"(m0v), "v"(voff), "s"(srd), "s"(soff), "i"(IMM) : "memory");
  else if constexpr (POL == 3)
    asm volatile("s_mov_b32 m0, %0\n\ts_nop 0\n\tbuffer_load_dwordx4 %1, %2, %3 offen offset:%4 sc0 sc1 lds"
                 :: "s"(m0v), "v"(voff), "s"(srd), "s"(soff), "i"(IMM) : "memory");
  else
    asm volatile("s_mov_b32 m0, %0\n\ts_nop 0\n\tbuffer_load_dwordx4 %1, %2, %3 offen offset:%4 sc1 nt lds"
                 :: "s"(m0v), "v"(voff), "s"(srd), "s"(soff), "i"(IMM) : "memory");
}
template <int IMM> DI void dma16_rt(unsigned pol, unsigned m0v, unsigned voff, __amdgpu_buffer_rsrc_t srd, unsigned soff) {
  asm volatile("s_mov_b32 m0, %0\n\ts_cmp_eq_u32 %5, 0\n\ts_cbranch_scc1 .Ldma_d%=\n\t"
               "buffer_load_dwordx4 %1, %2, %3 offen offset:%4 nt lds\n\ts_branch .Ldma_e%=\n"
               ".Ldma_d%=:\n\tbuffer_load_dwordx4 %1, %2, %3 offen offset:%4 lds\n.Ldma_e%=:"
               :: "s"(m0v), "v"(voff), "s"(srd), "s"(soff), "i"(IMM), "s"(pol) : "memory", "scc");
}
#define KV_POL 0
#define KV_POL2 KV_POL
#define GA_POL 0
#define GB_POL 0
DI bf16x8 lds_b128(unsigned a) { return *LDSP(const bf16x8, a); }
DI bf16x4 lds_tr(unsigned a) { return __builtin_amdgcn_ds_read_tr16_b64_v4bf16(LDSP(bf16x4, a)); }
DI bf16x8 cat8(bf16x4 a, bf16x4 b) { return __builtin_shufflevector(a, b, 0, 1, 2, 3, 4, 5, 6, 7); }

template <int I> struct IC { static constexpr int value = I; };
template <int B, int E, class F> DI void static_for(F&& f) {
  if constexpr (B < E) { f(IC<B>{}); static_for<B + 1, E>(static_cast<F&&>(f)); }
}

constexpr int G_NS = 4;
constexpr int G_STAGE = 32768;

template <int LDA, int LDB, int KLEN>
DI void gemm_tile(const __hip_bfloat16* __restrict__ Ag, unsigned Abytes, const __hip_bfloat16* __restrict__ Bg, unsigned Bbytes,
                  unsigned ldsbase, int rot, f32x16 (&acc)[2][2]) {
  constexpr int NT = KLEN / 64;
  static_assert((NT & (NT - 1)) == 0 && NT >= 4, "power-of-two tile count");
  const int tid = threadIdx.x, lane = tid & 63;
  const int wid = __builtin_amdgcn_readfirstlane(tid >> 6);
  if (wid >= 4) {
    const int lw = wid - 4;
    auto srdA = __builtin_amdgcn_make_buffer_rsrc((void*)Ag, 0, Abytes, 0x00020000);
    auto srdB = __builtin_amdgcn_make_buffer_rsrc((void*)Bg, 0, Bbytes, 0x00020000);
    const unsigned voffA0 = (unsigned)((lane >> 3) * LDA * 2 + ((((lane & 7) ^ ((lane >> 4) & 7))) << 4));
    const unsigned voffA1 = (unsigned)((lane >> 3) * LDA * 2 + ((((lane & 7) ^ ((4 + (lane >> 4)) & 7))) << 4));
    const unsigned voffB = (unsigned)((lane >> 4) * LDB * 2 + (((lane & 15) ^ (((lane >> 4) & 3) << 2)) << 4));
    auto issue = [&](int kt) {
      const int kr = (kt + rot) & (NT - 1);
      const unsigned mA = ldsbase + (unsigned)(kt & (G_NS - 1)) * G_STAGE + lw * 4096, mB = mA + 16384;
      const unsigned sA = (unsigned)(kr * 128 + lw * 4 * 16 * LDA), sB = (unsigned)((kr * 64 + lw * 16) * LDB * 2);
      dma16<0, GA_POL>(mA, voffA0, srdA, sA);
      dma16<0, GB_POL>(mB, voffB, srdB, sB);
      dma16<0, GA_POL>(mA + 1024, voffA1, srdA, sA + 16 * LDA);
      dma16<0, GB_POL>(mB + 1024, voffB, srdB, sB + 4 * LDB * 2);
      dma16<0, GA_POL>(mA + 2048, voffA0, srdA, sA + 32 * LDA);
      dma16<0, GB_POL>(mB + 2048, voffB, srdB, sB + 8 * LDB * 2);
      dma16<0, GA_POL>(mA + 3072, voffA1, srdA, sA + 48 * LDA);
      dma16<0, GB_POL>(mB + 3072, voffB, srdB, sB + 12 * LDB * 2);
    };
    issue(0); issue(1); issue(2);
    asm volatile("s_waitcnt vmcnt(16)\n\ts_barrier" ::: "memory");
    for (int kt = 0; kt < NT; ++kt) {
      if (kt + 2 < NT) asm volatile("s_waitcnt vmcnt(8)\n\ts_barrier" ::: "memory");
      else             asm volatile("s_waitcnt vmcnt(0)\n\ts_barrier" ::: "memory");
      if (kt + 3 < NT) issue(kt + 3);
    }
    return;
  }
  const int wm = wid >> 1, wn = wid & 1;
  const int m = lane & 31, h = lane >> 5;
  unsigned aaddr[4];
#pragma unroll
  for (int ks = 0; ks < 4; ++ks) aaddr[ks] = ldsbase + (unsigned)((wm * 64 + m) * 128 + (((2 * ks + h) ^ ((m >> 1) & 7)) << 4));
  const int g = lane >> 4, hh = g >> 1, ch = g & 1, q = (lane >> 2) & 3, p = lane & 3;
  unsigned baddr[2];
#pragma unroll
  for (int nb = 0; nb < 2; ++nb) baddr[nb] = ldsbase + 16384 + (unsigned)((8 * hh + q) * 256 + (((wn * 2 + nb) ^ q) << 6) + ch * 32 + p * 8);
#pragma unroll
  for (int i = 0; i < 2; ++i)
#pragma unroll
    for (int j = 0; j < 2; ++j) acc[i][j] = f32x16{};
  auto rdA = [&](unsigned stg, int ks, int mb) { return lds_b128(aaddr[ks] + stg + mb * 4096); };
  auto rdB = [&](unsigned stg, int ks, int nb) {
    return cat8(lds_tr(baddr[nb] + stg + (16 * ks) * 256), lds_tr(baddr[nb] + stg + (16 * ks + 4) * 256)); };
  asm volatile("s_barrier" ::: "memory");
  STAMP((LDA == DM_ ? 0 : 1), 1);
  bf16x8 af[2][2], bfr[2][2];
  af[0][0] = rdA(0, 0, 0); af[0][1] = rdA(0, 0, 1); bfr[0][0] = rdB(0, 0, 0); bfr[0][1] = rdB(0, 0, 1);
  unsigned stg = 0;
  for (int kt = 0; kt < NT; ++kt) {
    asm volatile("s_barrier" ::: "memory");
    const unsigned nstg = (stg + G_STAGE) & (G_NS * G_STAGE - 1);
    static_for<0, 16>([&](auto SS) {
      constexpr int s = decltype(SS)::value, ks = s >> 2, sub = s & 3, cur = ks & 1, nxt = cur ^ 1;
      constexpr int nb = sub >> 1, mb = sub & 1;
      acc[nb][mb] = mfma32(bfr[cur][nb], af[cur][mb], acc[nb][mb]);
      if constexpr (sub < 2) {
        if constexpr (ks < 3) { af[nxt][sub] = rdA(stg, ks + 1, sub); bfr[nxt][sub] = rdB(stg, ks + 1, sub); }
        else                  { af[nxt][sub] = rdA(nstg, 0, sub);     bfr[nxt][sub] = rdB(nstg, 0, sub); }
      }
      SBAR();
    });
    stg = nstg;
  }
}

__global__ __launch_bounds__(512, 2) void qproj_kernel(const __hip_bfloat16* __restrict__ x, const __hip_bfloat16* __restrict__ wq,
                                                       __hip_bfloat16* __restrict__ Qs) {
  __shared__ __attribute__((aligned(1024))) char lds[G_NS * G_STAGE];
  const unsigned ldsbase = (unsigned)(uintptr_t)LDSP(char, lds);
  const int bid = blockIdx.x, xcd = bid & 7, j = bid >> 3;
  const int nt = xcd * 4 + (j & 3), mt = j >> 2;
  STAMP(0, 0);
  f32x16 acc[2][2];
  gemm_tile<DM_, QCOLS, DM_>(x + (size_t)mt * 128 * DM_, (unsigned)((MROWS - mt * 128) * DM_ * 2),
                             wq + nt * 128, (unsigned)((DM_ * QCOLS - nt * 128) * 2), ldsbase, (2 * mt + 5 * (j & 3)) & 15, acc);
  STAMP(0, 2);
  const int lane = threadIdx.x & 63, wid = threadIdx.x >> 6, wm = wid >> 1, wn = wid & 1, m = lane & 31, h = lane >> 5;
  asm volatile("s_waitcnt lgkmcnt(0)\n\ts_barrier" ::: "memory");
  if (wid < 4) {
#pragma unroll
    for (int nb = 0; nb < 2; ++nb)
#pragma unroll
      for (int mb = 0; mb < 2; ++mb)
#pragma unroll
        for (int rq = 0; rq < 4; ++rq) {
          u32x2 w = {cvtpk(acc[nb][mb][4 * rq] * QSCALE, acc[nb][mb][4 * rq + 1] * QSCALE), cvtpk(acc[nb][mb][4 * rq + 2] * QSCALE, acc[nb][mb][4 * rq + 3] * QSCALE)};
          *LDSP(u32x2, ldsbase + (wm * 64 + mb * 32 + m) * 272 + (wn * 64 + nb * 32 + 8 * rq + 4 * h) * 2) = w;
        }
  }
  asm volatile("s_waitcnt lgkmcnt(0)\n\ts_barrier" ::: "memory");
  {
    const int sr = threadIdx.x & 31, ck = threadIdx.x >> 5;
#pragma unroll
    for (int bb = 0; bb < 4; ++bb) {
      u32x4 v = *LDSP(const u32x4, ldsbase + (bb * 32 + sr) * 272 + ck * 16);
      __builtin_nontemporal_store(v, reinterpret_cast<u32x4*>(Qs + ((((size_t)(mt * 4 + bb) * HQ_ + nt) * 16 + ck) * 32 + sr) * 8));
    }
  }
  STAMP(0, 3);
}

__global__ __launch_bounds__(512, 2) void oproj_kernel(const __hip_bfloat16* __restrict__ O, const __hip_bfloat16* __restrict__ wo,
                                                       float* __restrict__ part) {
  __shared__ __attribute__((aligned(1024))) char lds[G_NS * G_STAGE];
  const unsigned ldsbase = (unsigned)(uintptr_t)LDSP(char, lds);
  const int bid = blockIdx.x, xcd = bid & 7, j = bid >> 3;
  const int ksp = xcd >> 1, mt = (xcd & 1) * 4 + (j & 3), nt = j >> 2;
  STAMP(1, 0);
  f32x16 acc[2][2];
  gemm_tile<QCOLS, DM_, 1024>(O + (size_t)mt * 128 * QCOLS + ksp * 1024, (unsigned)(((MROWS - mt * 128) * QCOLS - ksp * 1024) * 2),
                              wo + (size_t)ksp * 1024 * DM_ + nt * 128, (unsigned)(((QCOLS - ksp * 1024) * DM_ - nt * 128) * 2), ldsbase,
                              (2 * nt + 5 * (j & 3)) & 15, acc);
  STAMP(1, 2);
  const int lane = threadIdx.x & 63, wid = threadIdx.x >> 6, wm = wid >> 1, wn = wid & 1, m = lane & 31, h = lane >> 5;
  if (wid < 4) {
    float* P = part + ((size_t)(ksp * 64 + mt * 8 + nt) * 4 + wid) * 4096 + lane * 4;
#pragma unroll
    for (int nb = 0; nb < 2; ++nb)
#pragma unroll
      for (int mb = 0; mb < 2; ++mb)
#pragma unroll
        for (int rq = 0; rq < 4; ++rq) {
          f32x4 v = {acc[nb][mb][4 * rq], acc[nb][mb][4 * rq + 1], acc[nb][mb][4 * rq + 2], acc[nb][mb][4 * rq + 3]};
          __builtin_nontemporal_store(v, reinterpret_cast<f32x4*>(P + ((nb * 2 + mb) * 4 + rq) * 256));
        }
  }
  STAMP(1, 3);
}

__global__ __launch_bounds__(256) void oreduce_kernel(const float* __restrict__ part, __hip_bfloat16* __restrict__ out) {
  const unsigned t = blockIdx.x * 256 + threadIdx.x;
  const unsigned lane = t & 63, rq = (t >> 6) & 3, blk = (t >> 8) & 3, wv = (t >> 10) & 3, tile = t >> 12;
  f32x4 a = *reinterpret_cast<const f32x4*>(part + (size_t)t * 4);
#pragma unroll
  for (int ks = 1; ks < 4; ++ks) a += *reinterpret_cast<const f32x4*>(part + ((size_t)ks * 262144 + t) * 4);
  const unsigned row = (tile >> 3) * 128 + (wv >> 1) * 64 + (blk & 1) * 32 + (lane & 31);
  const unsigned col = (tile & 7) * 128 + (wv & 1) * 64 + (blk >> 1) * 32 + 8 * rq + 4 * (lane >> 5);
  u32x2 w = {cvtpk(a[0], a[1]), cvtpk(a[2], a[3])};
  *reinterpret_cast<u32x2*>(out + (size_t)row * DM_ + col) = w;
}

constexpr int A_KOFF = 0, A_VOFF = 65536, A_STG = 32768, A_LDS = 131072;
constexpr float A_THR = 8.0f;
constexpr int A_NIT = SK_ / 128;

struct ACtx {
  f32x16 O[4];
  bf16x8 Q[8];
  f32x16 S[2][2];
  bf16x8 P[4];
  f32x16 NEGM;
  bf16x8 F[4];
  float m, l0, l1;
  float mx0, mx1, alpha;
  unsigned kaddr[8], vaddr[4];
  unsigned voffK[4], voffV, m0K[2], m0V[2], soffbase;
  unsigned voffR[2][4], m0R[2][2], tadd[2];
  __amdgpu_buffer_rsrc_t srdR[2];
  bool kfirst;
  unsigned polK, polV, tprev, tbase, tacc;
  __amdgpu_buffer_rsrc_t srdK, srdV;
};

template <int T, int KS, int VS> DI void a_read(ACtx& c) {
  if constexpr (T < 16) {
    constexpr int kb = T >> 3, step = T & 7;
    c.F[T & 3] = lds_b128(c.kaddr[step] + KS * A_STG + kb * 8192);
  } else {
    constexpr int u = T - 16, ks = u >> 2, db = u & 3;
    c.F[T & 3] = cat8(lds_tr(c.vaddr[db] + VS * A_STG + ks * 4096), lds_tr(c.vaddr[db] + VS * A_STG + ks * 4096 + 2048));
  }
}

template <int X, int P, bool DK, bool DV> DI void a_dma(ACtx& c, int it) {
  constexpr int R = P >> 2, D = P & 3;
  if constexpr ((R == 0 && DK) || (R == 1 && DV))
    dma16<D * 1024, (R == 0 ? KV_POL : KV_POL2)>(c.m0R[R][X], c.voffR[R][D], c.srdR[R], c.soffbase + ((unsigned)it + c.tadd[R]) * 32768u);
}
DI void a_adapt(ACtx& c, int it) {
  if (it < 7) {
    const unsigned now = __builtin_amdgcn_readfirstlane((unsigned)__builtin_amdgcn_s_memtime());
    const unsigned dt = now - c.tprev; c.tprev = now;
    if (it >= 1) c.tacc += dt;
    if (it == 2) { c.tbase = c.tacc; c.tacc = 0; c.polK = 1u; }
    if (it == 4) { const bool better = c.tacc * 32u < c.tbase * 31u; c.polK = better ? 1u : 0u; if (better) c.tbase = c.tacc; c.tacc = 0; c.polV = 1u; }
    if (it == 6) { c.polV = (c.tacc * 32u < c.tbase * 31u) ? 1u : 0u; }
    c.polK = __builtin_amdgcn_readfirstlane(c.polK); c.polV = __builtin_amdgcn_readfirstlane(c.polV);
    c.tacc = __builtin_amdgcn_readfirstlane(c.tacc); c.tbase = __builtin_amdgcn_readfirstlane(c.tbase);
  }
}

template <int XP, int G> DI void a_part2(ACtx& c) {
  if constexpr (G < 8) {
    float e0 = __builtin_amdgcn_exp2f(c.S[XP][1][2 * G]);     PIN(e0); c.S[XP][1][2 * G] = e0;
    float e1 = __builtin_amdgcn_exp2f(c.S[XP][1][2 * G + 1]); PIN(e1); c.S[XP][1][2 * G + 1] = e1;
  } else {
    constexpr int u = G - 8;
    c.l0 += c.S[XP][1][2 * u]; PIN(c.l0);
    c.l1 += c.S[XP][1][2 * u + 1]; PIN(c.l1);
    static_for<0, 2>([&](auto EE) {
      constexpr int Dw = 2 * u + decltype(EE)::value, ks = Dw >> 2, pd = Dw & 3;
      u32x4 w = __builtin_bit_cast(u32x4, c.P[ks]);
      unsigned pk = cvtpk(c.S[XP][ks >> 1][8 * (ks & 1) + 2 * pd], c.S[XP][ks >> 1][8 * (ks & 1) + 2 * pd + 1]); PIN(pk);
      w[pd] = pk;
      c.P[ks] = __builtin_bit_cast(bf16x8, w);
    });
  }
}

template <int X, int G, bool FIRST> DI void a_part1(ACtx& c, bool& pend) {
  f32x16 (&S)[2] = c.S[X];
  if constexpr (G < 4) {
    constexpr int kb = G >> 1, r = 8 * (G & 1);
    if constexpr (G == 0)
      asm volatile("v_max3_f32 %0, %2, %3, %4\n\tv_max3_f32 %1, %5, %6, %7\n\tv_max3_f32 %0, %0, %8, %9"
                   : "=&v"(c.mx0), "=&v"(c.mx1) : "v"(S[0][0]), "v"(S[0][1]), "v"(S[0][2]), "v"(S[0][3]), "v"(S[0][4]), "v"(S[0][5]), "v"(S[0][6]), "v"(S[0][7]));
    else
      asm volatile("v_max3_f32 %0, %0, %2, %3\n\tv_max3_f32 %1, %1, %4, %5\n\tv_max3_f32 %0, %0, %6, %7\n\tv_max3_f32 %1, %1, %8, %9"
                   : "+v"(c.mx0), "+v"(c.mx1) : "v"(S[kb][r]), "v"(S[kb][r + 1]), "v"(S[kb][r + 2]), "v"(S[kb][r + 3]), "v"(S[kb][r + 4]), "v"(S[kb][r + 5]), "v"(S[kb][r + 6]), "v"(S[kb][r + 7]));
  } else if constexpr (G == 4) {
    float mm = max2f(c.mx0, c.mx1);
    auto pr = __builtin_amdgcn_permlane32_swap(__float_as_uint(mm), __float_as_uint(mm), false, false);
    const float rmax = max2f(__uint_as_float(pr[0]), __uint_as_float(pr[1]));
    if constexpr (FIRST) {
      c.m = rmax;
#pragma unroll
      for (int r = 0; r < 16; ++r) { S[0][r] -= rmax; S[1][r] -= rmax; c.NEGM[r] = -rmax; }
    } else {
      if (__builtin_expect(__any(rmax > A_THR), 0)) {
        const float delta = fmaxf(rmax, 0.f);
        c.alpha = __builtin_amdgcn_exp2f(-delta);
        c.m += delta; c.l0 *= c.alpha; c.l1 *= c.alpha;
        const float nm = -c.m;
#pragma unroll
        for (int r = 0; r < 16; ++r) {
          S[0][r] -= delta; S[1][r] -= delta;
          asm volatile("v_mov_b32 %0, %1" : "+v"(c.NEGM[r]) : "v"(nm));
        }
        pend = true;
      }
    }
  } else {
    constexpr int u = G - 5;
    constexpr int e0 = (u * 16) / 11, e1 = ((u + 1) * 16) / 11;
    static_for<e0, e1>([&](auto EE) { constexpr int e = decltype(EE)::value; float t = __builtin_amdgcn_exp2f(S[0][e]); PIN(t); S[0][e] = t; });
    constexpr int a0 = u == 0 ? 0 : ((u - 1) * 16) / 11, a1 = u == 0 ? 0 : (G == 15 ? 16 : e0);
    static_for<a0, a1>([&](auto EE) { constexpr int e = decltype(EE)::value; if constexpr (e & 1) { c.l1 += S[0][e]; PIN(c.l1); } else { c.l0 += S[0][e]; PIN(c.l0); } });
  }
}

template <int X, bool QK, bool PV, bool FIRST, bool DK, bool DV, bool NEXTK, bool NDK, bool NDV>
DI void a_iter(ACtx& c, int it) {
  bool pend = false;
  static_for<0, 32>([&](auto SS) {
    constexpr int s = decltype(SS)::value;
    if constexpr (s == 29) {
      asm volatile("s_waitcnt vmcnt(0) lgkmcnt(0)\n\ts_barrier" ::: "memory");
    }
    if constexpr (A_ABL == 1) { } else
    if constexpr (s < 16) {
      if constexpr (QK) {
        constexpr int kb = s >> 3, step = s & 7;
        if constexpr (step == 0) {
          if constexpr (FIRST) c.S[X][kb] = mfma32(c.F[s & 3], c.Q[0], f32x16{});
          else                 c.S[X][kb] = mfma32(c.F[s & 3], c.Q[0], c.NEGM);
        } else c.S[X][kb] = mfma32(c.F[s & 3], c.Q[step], c.S[X][kb]);
      }
    } else {
      if constexpr (PV) { constexpr int u = s - 16, ks = u >> 2, db = u & 3; c.O[db] = mfma32(c.F[s & 3], c.P[ks], c.O[db]); }
    }
    constexpr int t = s + 3;
    if constexpr (A_ABL == 1) { } else
    if constexpr (t < 16) { if constexpr (QK) a_read<t, X, X ^ 1>(c); }
    else if constexpr (t < 32) { if constexpr (PV) a_read<t, X, X ^ 1>(c); }
    else { if constexpr (NEXTK) a_read<t - 32, X ^ 1, X>(c); }
    if constexpr (A_ABL != 2) {
      if constexpr (s < 5) a_dma<X, s + 3, DK, DV>(c, it);
      if constexpr (s >= 29) a_dma<X ^ 1, s - 29, NDK, NDV>(c, it + 1);
    }
    if constexpr (A_ABL == 1) { } else
    if constexpr (s < 16) { if constexpr (!FIRST) a_part2<X ^ 1, s>(c); }
    else { if constexpr (QK) a_part1<X, s - 16, FIRST>(c, pend); }
    SBAR();
  });
  if constexpr (QK && !FIRST && PV) {
    if (__builtin_expect(pend, 0)) {
#pragma unroll
      for (int d = 0; d < 4; ++d)
#pragma unroll
        for (int r = 0; r < 16; ++r) c.O[d][r] *= c.alpha;
    }
  }
}

__global__ __launch_bounds__(512, 2) void attn_kernel(const __hip_bfloat16* __restrict__ Qs, const __hip_bfloat16* __restrict__ kc,
                                                      const __hip_bfloat16* __restrict__ vc, __hip_bfloat16* __restrict__ Og) {
  __shared__ __attribute__((aligned(1024))) char lds[A_LDS];
  const unsigned ldsbase = (unsigned)(uintptr_t)LDSP(char, lds);
  const int bid = blockIdx.x, xcd = bid & 7, j = bid >> 3;
  const int pair = j >> 1, half = j & 1, hkv = xcd & 3, b = (xcd >> 2) * 16 + pair;
  const int tid = threadIdx.x, lane = tid & 63;
  const int wid = __builtin_amdgcn_readfirstlane(tid >> 6);
  const int rb = wid & 3, kh = wid >> 2;
  const int head = hkv * 8 + half * 4 + rb;
  STAMP(2, 0);
  ACtx c;
  const size_t kvoff = (size_t)(b * HKV_ + hkv) * SK_ * DH_;
  c.srdK = __builtin_amdgcn_make_buffer_rsrc((void*)(kc + kvoff), 0, SK_ * DH_ * 2, 0x00020000);
  c.srdV = __builtin_amdgcn_make_buffer_rsrc((void*)(vc + kvoff), 0, SK_ * DH_ * 2, 0x00020000);
  c.srdR[0] = c.srdK; c.srdR[1] = c.srdV;
  {
    const int jr = lane >> 4, sl = lane & 15;
#pragma unroll
    for (int k = 0; k < 4; ++k) c.voffK[k] = (unsigned)(jr * 256 + ((sl ^ ((4 * k + jr) & 15)) << 4));
    c.voffV = (unsigned)(jr * 256 + ((sl ^ (jr << 2)) << 4));
#pragma unroll
    for (int s = 0; s < 2; ++s) {
      c.m0K[s] = ldsbase + A_KOFF + s * A_STG + kh * 16384 + rb * 4096;
      c.m0V[s] = ldsbase + A_VOFF + s * A_STG + kh * 16384 + rb * 4096;
    }
    c.soffbase = (unsigned)(kh * 16384 + rb * 4096);
    c.kfirst = true;
#pragma unroll
    for (int r = 0; r < 2; ++r) {
      const bool isK = (r == 0) == c.kfirst;
#pragma unroll
      for (int k = 0; k < 4; ++k) c.voffR[r][k] = isK ? c.voffK[k] : c.voffV;
#pragma unroll
      for (int x = 0; x < 2; ++x) c.m0R[r][x] = isK ? c.m0K[x ^ 1] : c.m0V[x];
      c.tadd[r] = isK ? 1u : 0u;
    }
    const int kr = lane & 31, hi = lane >> 5;
#pragma unroll
    for (int st = 0; st < 8; ++st) c.kaddr[st] = ldsbase + A_KOFF + (unsigned)(kh * 16384 + kr * 256 + (((2 * st + hi) ^ (kr & 15)) << 4));
    const int g = lane >> 4, hh = g >> 1, ch = g & 1, q = (lane >> 2) & 3, p = lane & 3;
#pragma unroll
    for (int db = 0; db < 4; ++db) c.vaddr[db] = ldsbase + A_VOFF + (unsigned)(kh * 16384 + (4 * hh + q) * 256 + ((db ^ q) << 6) + ch * 32 + p * 8);
  }
#pragma unroll
  for (int i = 0; i < 8; ++i) PIN(c.kaddr[i]);
#pragma unroll
  for (int i = 0; i < 4; ++i) { PIN(c.vaddr[i]); PIN(c.voffK[i]); PIN(c.voffR[0][i]); PIN(c.voffR[1][i]); }
  PIN(c.voffV);
  dma16<0, KV_POL>(c.m0K[0], c.voffK[0], c.srdK, c.soffbase);
  dma16<1024, KV_POL>(c.m0K[0], c.voffK[1], c.srdK, c.soffbase);
  dma16<2048, KV_POL>(c.m0K[0], c.voffK[2], c.srdK, c.soffbase);
  dma16<3072, KV_POL>(c.m0K[0], c.voffK[3], c.srdK, c.soffbase);
  {
    const bf16x8* qp = reinterpret_cast<const bf16x8*>(Qs + ((size_t)(b * HQ_ + head) * 16 * 32 + lane) * 8);
#pragma unroll
    for (int st = 0; st < 8; ++st) asm volatile("global_load_dwordx4 %0, %1, off" : "=&v"(c.Q[st]) : "v"(qp + st * 64) : "memory");
  }
  dma16<0, KV_POL2>(c.m0V[0], c.voffV, c.srdV, c.soffbase);
  dma16<1024, KV_POL2>(c.m0V[0], c.voffV, c.srdV, c.soffbase);
  dma16<2048, KV_POL2>(c.m0V[0], c.voffV, c.srdV, c.soffbase);
  dma16<3072, KV_POL2>(c.m0V[0], c.voffV, c.srdV, c.soffbase);
  dma16<0, KV_POL>(c.m0K[1], c.voffK[0], c.srdK, c.soffbase + 32768u);
  dma16<1024, KV_POL>(c.m0K[1], c.voffK[1], c.srdK, c.soffbase + 32768u);
  dma16<2048, KV_POL>(c.m0K[1], c.voffK[2], c.srdK, c.soffbase + 32768u);
  dma16<3072, KV_POL>(c.m0K[1], c.voffK[3], c.srdK, c.soffbase + 32768u);
#pragma unroll
  for (int d = 0; d < 4; ++d) c.O[d] = f32x16{};
  c.l0 = 0.f; c.l1 = 0.f; c.m = 0.f; c.alpha = 1.f;
  c.polK = 0u; c.polV = 0u; c.tacc = 0u; c.tbase = 0u; c.tprev = 0u;
  asm volatile("s_waitcnt vmcnt(8)\n\ts_barrier" : "+v"(c.Q[0]), "+v"(c.Q[1]), "+v"(c.Q[2]), "+v"(c.Q[3]), "+v"(c.Q[4]), "+v"(c.Q[5]), "+v"(c.Q[6]), "+v"(c.Q[7]) :: "memory");
  STAMP(2, 1);
  a_read<0, 0, 1>(c); a_read<1, 0, 1>(c); a_read<2, 0, 1>(c);
  a_iter<0, true, false, true, false, false, true, true, true>(c, 0);
  for (int it = 1; it + 1 < A_NIT - 3; it += 2) {
    a_iter<1, true, true, false, true, true, true, true, true>(c, it);
    a_iter<0, true, true, false, true, true, true, true, true>(c, it + 1);
  }
  a_iter<1, true, true, false, true, true, true, true, true>(c, A_NIT - 3);
  a_iter<0, true, true, false, true, true, true, false, true>(c, A_NIT - 2);
  a_iter<1, true, true, false, false, true, false, false, false>(c, A_NIT - 1);
  a_iter<0, false, true, false, false, false, false, false, false>(c, A_NIT);

  STAMP(2, 2);
  float l = c.l0 + c.l1;
  { auto pr = __builtin_amdgcn_permlane32_swap(__float_as_uint(l), __float_as_uint(l), false, false);
    l = __uint_as_float(pr[0]) + __uint_as_float(pr[1]); }
  asm volatile("s_waitcnt lgkmcnt(0)\n\ts_barrier" ::: "memory");
  const unsigned xbase = ldsbase + rb * 17408;
  if (kh == 1) {
#pragma unroll
    for (int d = 0; d < 4; ++d)
#pragma unroll
      for (int rq = 0; rq < 4; ++rq) {
        f32x4 v = {c.O[d][4 * rq], c.O[d][4 * rq + 1], c.O[d][4 * rq + 2], c.O[d][4 * rq + 3]};
        *LDSP(f32x4, xbase + (d * 4 + rq) * 1024 + lane * 16) = v;
      }
    f32x2 ml = {c.m, l};
    *LDSP(f32x2, xbase + 16384 + lane * 8) = ml;
  }
  asm volatile("s_waitcnt lgkmcnt(0)\n\ts_barrier" ::: "memory");
  if (kh == 0) {
    const f32x2 ml = *LDSP(const f32x2, xbase + 16384 + lane * 8);
    const float mnew = fmaxf(c.m, ml[0]);
    const float f0 = __builtin_amdgcn_exp2f(c.m - mnew), f1 = __builtin_amdgcn_exp2f(ml[0] - mnew);
    const float inv = 1.0f / (f0 * l + f1 * ml[1]);
    const float g0 = f0 * inv, g1 = f1 * inv;
    const int qrow = lane & 31, hi = lane >> 5;
#pragma unroll
    for (int d = 0; d < 4; ++d)
#pragma unroll
      for (int rq = 0; rq < 4; ++rq) {
        const f32x4 v = *LDSP(const f32x4, xbase + (d * 4 + rq) * 1024 + lane * 16);
        const float o0 = c.O[d][4 * rq] * g0 + v[0] * g1, o1 = c.O[d][4 * rq + 1] * g0 + v[1] * g1;
        const float o2 = c.O[d][4 * rq + 2] * g0 + v[2] * g1, o3 = c.O[d][4 * rq + 3] * g0 + v[3] * g1;
        u32x2 w = {cvtpk(o0, o1), cvtpk(o2, o3)};
        c.O[d][4 * rq] = __uint_as_float(w[0]); c.O[d][4 * rq + 1] = __uint_as_float(w[1]);
      }
    asm volatile("s_waitcnt lgkmcnt(0)" ::: "memory");
#pragma unroll
    for (int d = 0; d < 4; ++d)
#pragma unroll
      for (int rq = 0; rq < 4; ++rq) {
        u32x2 w = {__float_as_uint(c.O[d][4 * rq]), __float_as_uint(c.O[d][4 * rq + 1])};
        *LDSP(u32x2, xbase + qrow * 272 + (32 * d + 8 * rq + 4 * hi) * 2) = w;
      }
    asm volatile("s_waitcnt lgkmcnt(0)" ::: "memory");
    __hip_bfloat16* og = Og + (size_t)(b * SQ_) * QCOLS + head * DH_;
#pragma unroll
    for (int it = 0; it < 8; ++it) {
      const int row = it * 4 + (lane >> 4), ck = lane & 15;
      u32x4 v = *LDSP(const u32x4, xbase + row * 272 + ck * 16);
      *reinterpret_cast<u32x4*>(og + (size_t)row * QCOLS + ck * 8) = v;
    }
  }
  STAMP(2, 3);
}

extern "C" void kernel_launch(void* const* d_in, const int* in_sizes, int n_in, void* d_out, int out_size, void* d_ws, size_t ws_size,
                              hipStream_t stream) {
  const __hip_bfloat16* x = (const __hip_bfloat16*)d_in[0];
  const __hip_bfloat16* kc = (const __hip_bfloat16*)d_in[1];
  const __hip_bfloat16* vc = (const __hip_bfloat16*)d_in[2];
  const __hip_bfloat16* wq = (const __hip_bfloat16*)d_in[3];
  const __hip_bfloat16* wo = (const __hip_bfloat16*)d_in[4];
  char* ws = (char*)d_ws;
  __hip_bfloat16* Qs = (__hip_bfloat16*)ws;
  __hip_bfloat16* Ob = (__hip_bfloat16*)(ws + (size_t)8 * 1024 * 1024);
  float* part = (float*)(ws + (size_t)16 * 1024 * 1024);
  qproj_kernel<<<256, 512, 0, stream>>>(x, wq, Qs);
  attn_kernel<<<256, 512, 0, stream>>>(Qs, kc, vc, Ob);
  oproj_kernel<<<256, 512, 0, stream>>>(Ob, wo, part);
  oreduce_kernel<<<1024, 256, 0, stream>>>(part, (__hip_bfloat16*)d_out);
}
```

```cpp
#include <hip/hip_runtime.h>
#include <hip/hip_bf16.h>
#include <stdint.h>

typedef __attribute__((ext_vector_type(8))) __bf16 bf16x8;
typedef __attribute__((ext_vector_type(4))) __bf16 bf16x4;
typedef __attribute__((ext_vector_type(2))) __bf16 bf16x2;
typedef __attribute__((ext_vector_type(16))) float f32x16;
typedef __attribute__((ext_vector_type(4))) float f32x4;
typedef __attribute__((ext_vector_type(2))) float f32x2;
typedef __attribute__((ext_vector_type(4))) unsigned u32x4;
typedef __attribute__((ext_vector_type(2))) unsigned u32x2;

#define DI __device__ __forceinline__
#define LDSP(T, a) ((__attribute__((address_space(3))) T*)(uintptr_t)(a))
#define SBAR() __builtin_amdgcn_sched_barrier(0)

constexpr int NB_ = 32, SQ_ = 32, DM_ = 1024, HQ_ = 32, HKV_ = 4, SK_ = 2048, DH_ = 128;
constexpr int MROWS = NB_ * SQ_;
constexpr int QCOLS = HQ_ * DH_;
constexpr float QSCALE = 0.08838834764831845f * 1.4426950408889634f;

DI unsigned cvtpk(float lo, float hi) {
  f32x2 v = {lo, hi}; bf16x2 b = __builtin_convertvector(v, bf16x2); return __builtin_bit_cast(unsigned, b);
}
DI float max2f(float a, float b) { float r; asm("v_max_f32_e32 %0, %1, %2" : "=v"(r) : "v"(a), "v"(b)); return r; }
#define PIN(x) asm volatile("" : "+v"(x))
DI f32x16 mfma32(bf16x8 a, bf16x8 b, f32x16 c) { return __builtin_amdgcn_mfma_f32_32x32x16_bf16(a, b, c, 0, 0, 0); }

template <int IMM, int POL = 0> DI void dma16(unsigned m0v, unsigned voff, __amdgpu_buffer_rsrc_t srd, unsigned soff) {
  if constexpr (POL == 0)
    asm volatile("s_mov_b32 m0, %0\n\ts_nop 0\n\tbuffer_load_dwordx4 %1, %2, %3 offen offset:%4 lds"
                 :: "s"(m0v), "v"(voff), "s"(srd), "s"(soff), "i"(IMM) : "memory");
  else
    asm volatile("s_mov_b32 m0, %0\n\ts_nop 0\n\tbuffer_load_dwordx4 %1, %2, %3 offen offset:%4 nt lds"
                 :: "s"(m0v), "v"(voff), "s"(srd), "s"(soff), "i"(IMM) : "memory");
}
constexpr int KV_POL = 0, KV_POL2 = 0, GA_POL = 0, GB_POL = 0;
DI bf16x8 lds_b128(unsigned a) { return *LDSP(const bf16x8, a); }
DI bf16x4 lds_tr(unsigned a) { return __builtin_amdgcn_ds_read_tr16_b64_v4bf16(LDSP(bf16x4, a)); }
DI bf16x8 cat8(bf16x4 a, bf16x4 b) { return __builtin_shufflevector(a, b, 0, 1, 2, 3, 4, 5, 6, 7); }

template <int I> struct IC { static constexpr int value = I; };
template <int B, int E, class F> DI void static_for(F&& f) {
  if constexpr (B < E) { f(IC<B>{}); static_for<B + 1, E>(static_cast<F&&>(f)); }
}

constexpr int G_NS = 4;
constexpr int G_STAGE = 32768;

template <int LDA, int LDB, int KLEN>
DI void gemm_tile(const __hip_bfloat16* __restrict__ Ag, unsigned Abytes, const __hip_bfloat16* __restrict__ Bg, unsigned Bbytes,
                  unsigned ldsbase, int rot, f32x16 (&acc)[2][2]) {
  constexpr int NT = KLEN / 64;
  static_assert((NT & (NT - 1)) == 0 && NT >= 4, "power-of-two tile count");
  const int tid = threadIdx.x, lane = tid & 63;
  const int wid = __builtin_amdgcn_readfirstlane(tid >> 6);
  if (wid >= 4) {
    const int lw = wid - 4;
    auto srdA = __builtin_amdgcn_make_buffer_rsrc((void*)Ag, 0, Abytes, 0x00020000);
    auto srdB = __builtin_amdgcn_make_buffer_rsrc((void*)Bg, 0, Bbytes, 0x00020000);
    const unsigned voffA0 = (unsigned)((lane >> 3) * LDA * 2 + ((((lane & 7) ^ ((lane >> 4) & 7))) << 4));
    const unsigned voffA1 = (unsigned)((lane >> 3) * LDA * 2 + ((((lane & 7) ^ ((4 + (lane >> 4)) & 7))) << 4));
    const unsigned voffB = (unsigned)((lane >> 4) * LDB * 2 + (((lane & 15) ^ (((lane >> 4) & 3) << 2)) << 4));
    auto issue = [&](int kt) {
      const int kr = (kt + rot) & (NT - 1);
      const unsigned mA = ldsbase + (unsigned)(kt & (G_NS - 1)) * G_STAGE + lw * 4096, mB = mA + 16384;
      const unsigned sA = (unsigned)(kr * 128 + lw * 4 * 16 * LDA), sB = (unsigned)((kr * 64 + lw * 16) * LDB * 2);
      dma16<0, GA_POL>(mA, voffA0, srdA, sA);
      dma16<0, GB_POL>(mB, voffB, srdB, sB);
      dma16<0, GA_POL>(mA + 1024, voffA1, srdA, sA + 16 * LDA);
      dma16<0, GB_POL>(mB + 1024, voffB, srdB, sB + 4 * LDB * 2);
      dma16<0, GA_POL>(mA + 2048, voffA0, srdA, sA + 32 * LDA);
      dma16<0, GB_POL>(mB + 2048, voffB, srdB, sB + 8 * LDB * 2);
      dma16<0, GA_POL>(mA + 3072, voffA1, srdA, sA + 48 * LDA);
      dma16<0, GB_POL>(mB + 3072, voffB, srdB, sB + 12 * LDB * 2);
    };
    issue(0); issue(1); issue(2);
    asm volatile("s_waitcnt vmcnt(16)\n\ts_barrier" ::: "memory");
    for (int kt = 0; kt < NT; ++kt) {
      if (kt + 2 < NT) asm volatile("s_waitcnt vmcnt(8)\n\ts_barrier" ::: "memory");
      else             asm volatile("s_waitcnt vmcnt(0)\n\ts_barrier" ::: "memory");
      if (kt + 3 < NT) issue(kt + 3);
    }
    return;
  }
  const int wm = wid >> 1, wn = wid & 1;
  const int m = lane & 31, h = lane >> 5;
  unsigned aaddr[4];
#pragma unroll
  for (int ks = 0; ks < 4; ++ks) aaddr[ks] = ldsbase + (unsigned)((wm * 64 + m) * 128 + (((2 * ks + h) ^ ((m >> 1) & 7)) << 4));
  const int g = lane >> 4, hh = g >> 1, ch = g & 1, q = (lane >> 2) & 3, p = lane & 3;
  unsigned baddr[2];
#pragma unroll
  for (int nb = 0; nb < 2; ++nb) baddr[nb] = ldsbase + 16384 + (unsigned)((8 * hh + q) * 256 + (((wn * 2 + nb) ^ q) << 6) + ch * 32 + p * 8);
#pragma unroll
  for (int i = 0; i < 2; ++i)
#pragma unroll
    for (int j = 0; j < 2; ++j) acc[i][j] = f32x16{};
  auto rdA = [&](unsigned stg, int ks, int mb) { return lds_b128(aaddr[ks] + stg + mb * 4096); };
  auto rdB = [&](unsigned stg, int ks, int nb) {
    return cat8(lds_tr(baddr[nb] + stg + (16 * ks) * 256), lds_tr(baddr[nb] + stg + (16 * ks + 4) * 256)); };
  asm volatile("s_barrier" ::: "memory");
  bf16x8 af[2][2], bfr[2][2];
  af[0][0] = rdA(0, 0, 0); af[0][1] = rdA(0, 0, 1); bfr[0][0] = rdB(0, 0, 0); bfr[0][1] = rdB(0, 0, 1);
  unsigned stg = 0;
  for (int kt = 0; kt < NT; ++kt) {
    asm volatile("s_barrier" ::: "memory");
    const unsigned nstg = (stg + G_STAGE) & (G_NS * G_STAGE - 1);
    static_for<0, 16>([&](auto SS) {
      constexpr int s = decltype(SS)::value, ks = s >> 2, sub = s & 3, cur = ks & 1, nxt = cur ^ 1;
      constexpr int nb = sub >> 1, mb = sub & 1;
      acc[nb][mb] = mfma32(bfr[cur][nb], af[cur][mb], acc[nb][mb]);
      if constexpr (sub < 2) {
        if constexpr (ks < 3) { af[nxt][sub] = rdA(stg, ks + 1, sub); bfr[nxt][sub] = rdB(stg, ks + 1, sub); }
        else                  { af[nxt][sub] = rdA(nstg, 0, sub);     bfr[nxt][sub] = rdB(nstg, 0, sub); }
      }
      SBAR();
    });
    stg = nstg;
  }
}


__global__ __launch_bounds__(512, 2) void qproj_kernel(const __hip_bfloat16* __restrict__ x, const __hip_bfloat16* __restrict__ wq,
                                                       __hip_bfloat16* __restrict__ Qs) {
  __shared__ __attribute__((aligned(1024))) char lds[G_NS * G_STAGE];
  const unsigned ldsbase = (unsigned)(uintptr_t)LDSP(char, lds);
  const int bid = blockIdx.x, xcd = bid & 7, j = bid >> 3;
  const int nt = xcd * 4 + (j & 3), mt = j >> 2;
  f32x16 acc[2][2];
  gemm_tile<DM_, QCOLS, DM_>(x + (size_t)mt * 128 * DM_, (unsigned)((MROWS - mt * 128) * DM_ * 2),
                             wq + nt * 128, (unsigned)((DM_ * QCOLS - nt * 128) * 2), ldsbase, (2 * mt + 5 * (j & 3)) & 15, acc);
  const int lane = threadIdx.x & 63, wid = threadIdx.x >> 6, wm = wid >> 1, wn = wid & 1, m = lane & 31, h = lane >> 5;
  asm volatile("s_waitcnt lgkmcnt(0)\n\ts_barrier" ::: "memory");
  if (wid < 4) {
#pragma unroll
    for (int nb = 0; nb < 2; ++nb)
#pragma unroll
      for (int mb = 0; mb < 2; ++mb)
#pragma unroll
        for (int rq = 0; rq < 4; ++rq) {
          u32x2 w = {cvtpk(acc[nb][mb][4 * rq] * QSCALE, acc[nb][mb][4 * rq + 1] * QSCALE), cvtpk(acc[nb][mb][4 * rq + 2] * QSCALE, acc[nb][mb][4 * rq + 3] * QSCALE)};
          *LDSP(u32x2, ldsbase + (wm * 64 + mb * 32 + m) * 272 + (wn * 64 + nb * 32 + 8 * rq + 4 * h) * 2) = w;
        }
  }
  asm volatile("s_waitcnt lgkmcnt(0)\n\ts_barrier" ::: "memory");
  {
    const int sr = threadIdx.x & 31, ck = threadIdx.x >> 5;
#pragma unroll
    for (int bb = 0; bb < 4; ++bb) {
      u32x4 v = *LDSP(const u32x4, ldsbase + (bb * 32 + sr) * 272 + ck * 16);
      __builtin_nontemporal_store(v, reinterpret_cast<u32x4*>(Qs + ((((size_t)(mt * 4 + bb) * HQ_ + nt) * 16 + ck) * 32 + sr) * 8));
    }
  }
}

__global__ __launch_bounds__(512, 2) void oproj_kernel(const __hip_bfloat16* __restrict__ O, const __hip_bfloat16* __restrict__ wo,
                                                       float* __restrict__ part) {
  __shared__ __attribute__((aligned(1024))) char lds[G_NS * G_STAGE];
  const unsigned ldsbase = (unsigned)(uintptr_t)LDSP(char, lds);
  const int bid = blockIdx.x, xcd = bid & 7, j = bid >> 3;
  const int ksp = xcd >> 1, mt = (xcd & 1) * 4 + (j & 3), nt = j >> 2;
  f32x16 acc[2][2];
  gemm_tile<QCOLS, DM_, 1024>(O + (size_t)mt * 128 * QCOLS + ksp * 1024, (unsigned)(((MROWS - mt * 128) * QCOLS - ksp * 1024) * 2),
                              wo + (size_t)ksp * 1024 * DM_ + nt * 128, (unsigned)(((QCOLS - ksp * 1024) * DM_ - nt * 128) * 2), ldsbase,
                              (2 * nt + 5 * (j & 3)) & 15, acc);
  const int lane = threadIdx.x & 63, wid = threadIdx.x >> 6, wm = wid >> 1, wn = wid & 1, m = lane & 31, h = lane >> 5;
  if (wid < 4) {
    float* P = part + ((size_t)(ksp * 64 + mt * 8 + nt) * 4 + wid) * 4096 + lane * 4;
#pragma unroll
    for (int nb = 0; nb < 2; ++nb)
#pragma unroll
      for (int mb = 0; mb < 2; ++mb)
#pragma unroll
        for (int rq = 0; rq < 4; ++rq) {
          f32x4 v = {acc[nb][mb][4 * rq], acc[nb][mb][4 * rq + 1], acc[nb][mb][4 * rq + 2], acc[nb][mb][4 * rq + 3]};
          __builtin_nontemporal_store(v, reinterpret_cast<f32x4*>(P + ((nb * 2 + mb) * 4 + rq) * 256));
        }
  }
}

__global__ __launch_bounds__(256) void oreduce_kernel(const float* __restrict__ part, __hip_bfloat16* __restrict__ out) {
  const unsigned t = blockIdx.x * 256 + threadIdx.x;
  const unsigned lane = t & 63, rq = (t >> 6) & 3, blk = (t >> 8) & 3, wv = (t >> 10) & 3, tile = t >> 12;
  f32x4 a = *reinterpret_cast<const f32x4*>(part + (size_t)t * 4);
#pragma unroll
  for (int ks = 1; ks < 4; ++ks) a += *reinterpret_cast<const f32x4*>(part + ((size_t)ks * 262144 + t) * 4);
  const unsigned row = (tile >> 3) * 128 + (wv >> 1) * 64 + (blk & 1) * 32 + (lane & 31);
  const unsigned col = (tile & 7) * 128 + (wv & 1) * 64 + (blk >> 1) * 32 + 8 * rq + 4 * (lane >> 5);
  u32x2 w = {cvtpk(a[0], a[1]), cvtpk(a[2], a[3])};
  *reinterpret_cast<u32x2*>(out + (size_t)row * DM_ + col) = w;
}

constexpr int A_KOFF = 0, A_VOFF = 65536, A_STG = 32768, A_LDS = 131072;
constexpr float A_THR = 8.0f;
constexpr int A_NIT = SK_ / 128;

struct ACtx {
  f32x16 O[4];
  bf16x8 Q[8];
  f32x16 S[2][2];
  bf16x8 P[4];
  f32x16 NEGM;
  bf16x8 F[4];
  float m, l0, l1;
  float mx0, mx1, alpha;
  unsigned kaddr[8], vaddr[4];
  unsigned voffK[4], voffV, m0K[2], m0V[2], soffbase;
  unsigned voffR[2][4], m0R[2][2], tadd[2];
  __amdgpu_buffer_rsrc_t srdR[2];
  bool kfirst;
  __amdgpu_buffer_rsrc_t srdK, srdV;
};

template <int T, int KS, int VS> DI void a_read(ACtx& c) {
  if constexpr (T < 16) {
    constexpr int kb = T >> 3, step = T & 7;
    c.F[T & 3] = lds_b128(c.kaddr[step] + KS * A_STG + kb * 8192);
  } else {
    constexpr int u = T - 16, ks = u >> 2, db = u & 3;
    c.F[T & 3] = cat8(lds_tr(c.vaddr[db] + VS * A_STG + ks * 4096), lds_tr(c.vaddr[db] + VS * A_STG + ks * 4096 + 2048));
  }
}

template <int X, int P, bool DK, bool DV> DI void a_dma(ACtx& c, int it) {
  constexpr int R = P >> 2, D = P & 3;
  if constexpr ((R == 0 && DK) || (R == 1 && DV))
    dma16<D * 1024, (R == 0 ? KV_POL : KV_POL2)>(c.m0R[R][X], c.voffR[R][D], c.srdR[R], c.soffbase + ((unsigned)it + c.tadd[R]) * 32768u);
}
template <int XP, int G> DI void a_part2(ACtx& c) {
  if constexpr (G < 8) {
    float e0 = __builtin_amdgcn_exp2f(c.S[XP][1][2 * G]);     PIN(e0); c.S[XP][1][2 * G] = e0;
    float e1 = __builtin_amdgcn_exp2f(c.S[XP][1][2 * G + 1]); PIN(e1); c.S[XP][1][2 * G + 1] = e1;
  } else {
    constexpr int u = G - 8;
    c.l0 += c.S[XP][1][2 * u]; PIN(c.l0);
    c.l1 += c.S[XP][1][2 * u + 1]; PIN(c.l1);
    static_for<0, 2>([&](auto EE) {
      constexpr int Dw = 2 * u + decltype(EE)::value, ks = Dw >> 2, pd = Dw & 3;
      u32x4 w = __builtin_bit_cast(u32x4, c.P[ks]);
      unsigned pk = cvtpk(c.S[XP][ks >> 1][8 * (ks & 1) + 2 * pd], c.S[XP][ks >> 1][8 * (ks & 1) + 2 * pd + 1]); PIN(pk);
      w[pd] = pk;
      c.P[ks] = __builtin_bit_cast(bf16x8, w);
    });
  }
}

template <int X, int G, bool FIRST> DI void a_part1(ACtx& c, bool& pend) {
  f32x16 (&S)[2] = c.S[X];
  if constexpr (G < 4) {
    constexpr int kb = G >> 1, r = 8 * (G & 1);
    if constexpr (G == 0)
      asm volatile("v_max3_f32 %0, %2, %3, %4\n\tv_max3_f32 %1, %5, %6, %7\n\tv_max3_f32 %0, %0, %8, %9"
                   : "=&v"(c.mx0), "=&v"(c.mx1) : "v"(S[0][0]), "v"(S[0][1]), "v"(S[0][2]), "v"(S[0][3]), "v"(S[0][4]), "v"(S[0][5]), "v"(S[0][6]), "v"(S[0][7]));
    else
      asm volatile("v_max3_f32 %0, %0, %2, %3\n\tv_max3_f32 %1, %1, %4, %5\n\tv_max3_f32 %0, %0, %6, %7\n\tv_max3_f32 %1, %1, %8, %9"
                   : "+v"(c.mx0), "+v"(c.mx1) : "v"(S[kb][r]), "v"(S[kb][r + 1]), "v"(S[kb][r + 2]), "v"(S[kb][r + 3]), "v"(S[kb][r + 4]), "v"(S[kb][r + 5]), "v"(S[kb][r + 6]), "v"(S[kb][r + 7]));
  } else if constexpr (G == 4) {
    float mm = max2f(c.mx0, c.mx1);
    auto pr = __builtin_amdgcn_permlane32_swap(__float_as_uint(mm), __float_as_uint(mm), false, false);
    const float rmax = max2f(__uint_as_float(pr[0]), __uint_as_float(pr[1]));
    if constexpr (FIRST) {
      c.m = rmax;
#pragma unroll
      for (int r = 0; r < 16; ++r) { S[0][r] -= rmax; S[1][r] -= rmax; c.NEGM[r] = -rmax; }
    } else {
      if (__builtin_expect(__any(rmax > A_THR), 0)) {
        const float delta = fmaxf(rmax, 0.f);
        c.alpha = __builtin_amdgcn_exp2f(-delta);
        c.m += delta; c.l0 *= c.alpha; c.l1 *= c.alpha;
        const float nm = -c.m;
#pragma unroll
        for (int r = 0; r < 16; ++r) {
          S[0][r] -= delta; S[1][r] -= delta;
          asm volatile("v_mov_b32 %0, %1" : "+v"(c.NEGM[r]) : "v"(nm));
        }
        pend = true;
      }
    }
  } else {
    constexpr int u = G - 5;
    constexpr int e0 = (u * 16) / 11, e1 = ((u + 1) * 16) / 11;
    static_for<e0, e1>([&](auto EE) { constexpr int e = decltype(EE)::value; float t = __builtin_amdgcn_exp2f(S[0][e]); PIN(t); S[0][e] = t; });
    constexpr int a0 = u == 0 ? 0 : ((u - 1) * 16) / 11, a1 = u == 0 ? 0 : (G == 15 ? 16 : e0);
    static_for<a0, a1>([&](auto EE) { constexpr int e = decltype(EE)::value; if constexpr (e & 1) { c.l1 += S[0][e]; PIN(c.l1); } else { c.l0 += S[0][e]; PIN(c.l0); } });
  }
}

template <int X, bool QK, bool PV, bool FIRST, bool DK, bool DV, bool NEXTK, bool NDK, bool NDV>
DI void a_iter(ACtx& c, int it) {
  bool pend = false;
  static_for<0, 32>([&](auto SS) {
    constexpr int s = decltype(SS)::value;
    if constexpr (s == 29) {
      asm volatile("s_waitcnt vmcnt(0) lgkmcnt(0)\n\ts_barrier" ::: "memory");
    }
    if constexpr (s < 16) {
      if constexpr (QK) {
        constexpr int kb = s >> 3, step = s & 7;
        if constexpr (step == 0) {
          if constexpr (FIRST) c.S[X][kb] = mfma32(c.F[s & 3], c.Q[0], f32x16{});
          else                 c.S[X][kb] = mfma32(c.F[s & 3], c.Q[0], c.NEGM);
        } else c.S[X][kb] = mfma32(c.F[s & 3], c.Q[step], c.S[X][kb]);
      }
    } else {
      if constexpr (PV) { constexpr int u = s - 16, ks = u >> 2, db = u & 3; c.O[db] = mfma32(c.F[s & 3], c.P[ks], c.O[db]); }
    }
    constexpr int t = s + 3;
    if constexpr (t < 16) { if constexpr (QK) a_read<t, X, X ^ 1>(c); }
    else if constexpr (t < 32) { if constexpr (PV) a_read<t, X, X ^ 1>(c); }
    else { if constexpr (NEXTK) a_read<t - 32, X ^ 1, X>(c); }
    {
      if constexpr (s < 5) a_dma<X, s + 3, DK, DV>(c, it);
      if constexpr (s >= 29) a_dma<X ^ 1, s - 29, NDK, NDV>(c, it + 1);
    }
    if constexpr (s < 16) { if constexpr (!FIRST) a_part2<X ^ 1, s>(c); }
    else { if constexpr (QK) a_part1<X, s - 16, FIRST>(c, pend); }
    SBAR();
  });
  if constexpr (QK && !FIRST && PV) {
    if (__builtin_expect(pend, 0)) {
#pragma unroll
      for (int d = 0; d < 4; ++d)
#pragma unroll
        for (int r = 0; r < 16; ++r) c.O[d][r] *= c.alpha;
    }
  }
}

__global__ __launch_bounds__(512, 2) void attn_kernel(const __hip_bfloat16* __restrict__ Qs, const __hip_bfloat16* __restrict__ kc,
                                                      const __hip_bfloat16* __restrict__ vc, __hip_bfloat16* __restrict__ Og) {
  __shared__ __attribute__((aligned(1024))) char lds[A_LDS];
  const unsigned ldsbase = (unsigned)(uintptr_t)LDSP(char, lds);
  const int bid = blockIdx.x, xcd = bid & 7, j = bid >> 3;
  const int pair = j >> 1, half = j & 1, hkv = xcd & 3, b = (xcd >> 2) * 16 + pair;
  const int tid = threadIdx.x, lane = tid & 63;
  const int wid = __builtin_amdgcn_readfirstlane(tid >> 6);
  const int rb = wid & 3, kh = wid >> 2;
  const int head = hkv * 8 + half * 4 + rb;
  ACtx c;
  const size_t kvoff = (size_t)(b * HKV_ + hkv) * SK_ * DH_;
  c.srdK = __builtin_amdgcn_make_buffer_rsrc((void*)(kc + kvoff), 0, SK_ * DH_ * 2, 0x00020000);
  c.srdV = __builtin_amdgcn_make_buffer_rsrc((void*)(vc + kvoff), 0, SK_ * DH_ * 2, 0x00020000);
  c.srdR[0] = c.srdK; c.srdR[1] = c.srdV;
  {
    const int jr = lane >> 4, sl = lane & 15;
#pragma unroll
    for (int k = 0; k < 4; ++k) c.voffK[k] = (unsigned)(jr * 256 + ((sl ^ ((4 * k + jr) & 15)) << 4));
    c.voffV = (unsigned)(jr * 256 + ((sl ^ (jr << 2)) << 4));
#pragma unroll
    for (int s = 0; s < 2; ++s) {
      c.m0K[s] = ldsbase + A_KOFF + s * A_STG + kh * 16384 + rb * 4096;
      c.m0V[s] = ldsbase + A_VOFF + s * A_STG + kh * 16384 + rb * 4096;
    }
    c.soffbase = (unsigned)(kh * 16384 + rb * 4096);
    c.kfirst = true;
#pragma unroll
    for (int r = 0; r < 2; ++r) {
      const bool isK = (r == 0) == c.kfirst;
#pragma unroll
      for (int k = 0; k < 4; ++k) c.voffR[r][k] = isK ? c.voffK[k] : c.voffV;
#pragma unroll
      for (int x = 0; x < 2; ++x) c.m0R[r][x] = isK ? c.m0K[x ^ 1] : c.m0V[x];
      c.tadd[r] = isK ? 1u : 0u;
    }
    const int kr = lane & 31, hi = lane >> 5;
#pragma unroll
    for (int st = 0; st < 8; ++st) c.kaddr[st] = ldsbase + A_KOFF + (unsigned)(kh * 16384 + kr * 256 + (((2 * st + hi) ^ (kr & 15)) << 4));
    const int g = lane >> 4, hh = g >> 1, ch = g & 1, q = (lane >> 2) & 3, p = lane & 3;
#pragma unroll
    for (int db = 0; db < 4; ++db) c.vaddr[db] = ldsbase + A_VOFF + (unsigned)(kh * 16384 + (4 * hh + q) * 256 + ((db ^ q) << 6) + ch * 32 + p * 8);
  }
#pragma unroll
  for (int i = 0; i < 8; ++i) PIN(c.kaddr[i]);
#pragma unroll
  for (int i = 0; i < 4; ++i) { PIN(c.vaddr[i]); PIN(c.voffK[i]); PIN(c.voffR[0][i]); PIN(c.voffR[1][i]); }
  PIN(c.voffV);
  dma16<0, KV_POL>(c.m0K[0], c.voffK[0], c.srdK, c.soffbase);
  dma16<1024, KV_POL>(c.m0K[0], c.voffK[1], c.srdK, c.soffbase);
  dma16<2048, KV_POL>(c.m0K[0], c.voffK[2], c.srdK, c.soffbase);
  dma16<3072, KV_POL>(c.m0K[0], c.voffK[3], c.srdK, c.soffbase);
  {
    const bf16x8* qp = reinterpret_cast<const bf16x8*>(Qs + ((size_t)(b * HQ_ + head) * 16 * 32 + lane) * 8);
#pragma unroll
    for (int st = 0; st < 8; ++st) asm volatile("global_load_dwordx4 %0, %1, off" : "=&v"(c.Q[st]) : "v"(qp + st * 64) : "memory");
  }
  dma16<0, KV_POL2>(c.m0V[0], c.voffV, c.srdV, c.soffbase);
  dma16<1024, KV_POL2>(c.m0V[0], c.voffV, c.srdV, c.soffbase);
  dma16<2048, KV_POL2>(c.m0V[0], c.voffV, c.srdV, c.soffbase);
  dma16<3072, KV_POL2>(c.m0V[0], c.voffV, c.srdV, c.soffbase);
  dma16<0, KV_POL>(c.m0K[1], c.voffK[0], c.srdK, c.soffbase + 32768u);
  dma16<1024, KV_POL>(c.m0K[1], c.voffK[1], c.srdK, c.soffbase + 32768u);
  dma16<2048, KV_POL>(c.m0K[1], c.voffK[2], c.srdK, c.soffbase + 32768u);
  dma16<3072, KV_POL>(c.m0K[1], c.voffK[3], c.srdK, c.soffbase + 32768u);
#pragma unroll
  for (int d = 0; d < 4; ++d) c.O[d] = f32x16{};
  c.l0 = 0.f; c.l1 = 0.f; c.m = 0.f; c.alpha = 1.f;
  asm volatile("s_waitcnt vmcnt(8)\n\ts_barrier" : "+v"(c.Q[0]), "+v"(c.Q[1]), "+v"(c.Q[2]), "+v"(c.Q[3]), "+v"(c.Q[4]), "+v"(c.Q[5]), "+v"(c.Q[6]), "+v"(c.Q[7]) :: "memory");
  a_read<0, 0, 1>(c); a_read<1, 0, 1>(c); a_read<2, 0, 1>(c);
  a_iter<0, true, false, true, false, false, true, true, true>(c, 0);
  for (int it = 1; it + 1 < A_NIT - 3; it += 2) {
    a_iter<1, true, true, false, true, true, true, true, true>(c, it);
    a_iter<0, true, true, false, true, true, true, true, true>(c, it + 1);
  }
  a_iter<1, true, true, false, true, true, true, true, true>(c, A_NIT - 3);
  a_iter<0, true, true, false, true, true, true, false, true>(c, A_NIT - 2);
  a_iter<1, true, true, false, false, true, false, false, false>(c, A_NIT - 1);
  a_iter<0, false, true, false, false, false, false, false, false>(c, A_NIT);
  float l = c.l0 + c.l1;
  { auto pr = __builtin_amdgcn_permlane32_swap(__float_as_uint(l), __float_as_uint(l), false, false);
    l = __uint_as_float(pr[0]) + __uint_as_float(pr[1]); }
  asm volatile("s_waitcnt lgkmcnt(0)\n\ts_barrier" ::: "memory");
  const unsigned xbase = ldsbase + rb * 17408;
  if (kh == 1) {
#pragma unroll
    for (int d = 0; d < 4; ++d)
#pragma unroll
      for (int rq = 0; rq < 4; ++rq) {
        f32x4 v = {c.O[d][4 * rq], c.O[d][4 * rq + 1], c.O[d][4 * rq + 2], c.O[d][4 * rq + 3]};
        *LDSP(f32x4, xbase + (d * 4 + rq) * 1024 + lane * 16) = v;
      }
    f32x2 ml = {c.m, l};
    *LDSP(f32x2, xbase + 16384 + lane * 8) = ml;
  }
  asm volatile("s_waitcnt lgkmcnt(0)\n\ts_barrier" ::: "memory");
  if (kh == 0) {
    const f32x2 ml = *LDSP(const f32x2, xbase + 16384 + lane * 8);
    const float mnew = fmaxf(c.m, ml[0]);
    const float f0 = __builtin_amdgcn_exp2f(c.m - mnew), f1 = __builtin_amdgcn_exp2f(ml[0] - mnew);
    const float inv = 1.0f / (f0 * l + f1 * ml[1]);
    const float g0 = f0 * inv, g1 = f1 * inv;
    const int qrow = lane & 31, hi = lane >> 5;
#pragma unroll
    for (int d = 0; d < 4; ++d)
#pragma unroll
      for (int rq = 0; rq < 4; ++rq) {
        const f32x4 v = *LDSP(const f32x4, xbase + (d * 4 + rq) * 1024 + lane * 16);
        const float o0 = c.O[d][4 * rq] * g0 + v[0] * g1, o1 = c.O[d][4 * rq + 1] * g0 + v[1] * g1;
        const float o2 = c.O[d][4 * rq + 2] * g0 + v[2] * g1, o3 = c.O[d][4 * rq + 3] * g0 + v[3] * g1;
        u32x2 w = {cvtpk(o0, o1), cvtpk(o2, o3)};
        c.O[d][4 * rq] = __uint_as_float(w[0]); c.O[d][4 * rq + 1] = __uint_as_float(w[1]);
      }
    asm volatile("s_waitcnt lgkmcnt(0)" ::: "memory");
#pragma unroll
    for (int d = 0; d < 4; ++d)
#pragma unroll
      for (int rq = 0; rq < 4; ++rq) {
        u32x2 w = {__float_as_uint(c.O[d][4 * rq]), __float_as_uint(c.O[d][4 * rq + 1])};
        *LDSP(u32x2, xbase + qrow * 272 + (32 * d + 8 * rq + 4 * hi) * 2) = w;
      }
    asm volatile("s_waitcnt lgkmcnt(0)" ::: "memory");
    __hip_bfloat16* og = Og + (size_t)(b * SQ_) * QCOLS + head * DH_;
#pragma unroll
    for (int it = 0; it < 8; ++it) {
      const int row = it * 4 + (lane >> 4), ck = lane & 15;
      u32x4 v = *LDSP(const u32x4, xbase + row * 272 + ck * 16);
      *reinterpret_cast<u32x4*>(og + (size_t)row * QCOLS + ck * 8) = v;
    }
  }
}

extern "C" void kernel_launch(void* const* d_in, const int* in_sizes, int n_in, void* d_out, int out_size, void* d_ws, size_t ws_size,
                              hipStream_t stream) {
  const __hip_bfloat16* x = (const __hip_bfloat16*)d_in[0];
  const __hip_bfloat16* kc = (const __hip_bfloat16*)d_in[1];
  const __hip_bfloat16* vc = (const __hip_bfloat16*)d_in[2];
  const __hip_bfloat16* wq = (const __hip_bfloat16*)d_in[3];
  const __hip_bfloat16* wo = (const __hip_bfloat16*)d_in[4];
  char* ws = (char*)d_ws;
  __hip_bfloat16* Qs = (__hip_bfloat16*)ws;
  __hip_bfloat16* Ob = (__hip_bfloat16*)(ws + (size_t)8 * 1024 * 1024);
  float* part = (float*)(ws + (size_t)16 * 1024 * 1024);
  qproj_kernel<<<256, 512, 0, stream>>>(x, wq, Qs);
  attn_kernel<<<256, 512, 0, stream>>>(Qs, kc, vc, Ob);
  oproj_kernel<<<256, 512, 0, stream>>>(Ob, wo, part);
  oreduce_kernel<<<1024, 256, 0, stream>>>(part, (__hip_bfloat16*)d_out);
}
```
